```python
import torch

def _softmax(x, axis=-1):
    return torch.softmax(x, dim=axis)
def _gelu(x):
    return torch.nn.functional.gelu(x, approximate="tanh")
def _silu(x):
    return torch.nn.functional.silu(x)
def _sigmoid(x):
    return torch.sigmoid(x)
def _layernorm(x, axis=-1, eps=1e-5):
    m = x.mean(dim=axis, keepdim=True)
    v = x.var(dim=axis, keepdim=True, unbiased=False)
    return (x - m) / torch.sqrt(v + eps)
def _scatter_add(tgt, idx, src, axis=0):
    return tgt.clone().index_add_(axis, idx, src)
def _segment_sum(src, seg, n):
    out = torch.zeros((n,) + src.shape[1:], dtype=src.dtype)
    return out.index_add_(0, seg, src)
def _pad(x, pw):
    flat = tuple(v for pair in reversed(pw) for v in pair)
    return torch.nn.functional.pad(x, flat)

kernel_name = 'standalone__gqa_block_cached__near_ridge'

def setup_inputs():

    _w = (lambda a: (lambda t: t.to(torch.bfloat16) if t.is_floating_point() else t)(torch.as_tensor(a)))
    B, Sq, D = 8, 48, 512
    Hq, Hkv, Sk, Dh = 32, 8, 2048, 128
    assert Hq % Hkv == 0, (Hq, Hkv)
    return {
        "x": _w((lambda *s: torch.randn(*s, dtype=torch.bfloat16) * 0.8)(B, Sq, D)),
        "k_cache": _w((lambda *s: torch.randn(*s, dtype=torch.bfloat16) * 0.8)(B, Hkv, Sk, Dh)),
        "v_cache": _w((lambda *s: torch.randn(*s, dtype=torch.bfloat16) * 0.05)(B, Hkv, Sk, Dh)),
        "wq": _w((lambda *s: torch.randn(*s, dtype=torch.bfloat16) * 0.8)(D, Hq * Dh) * (D ** -0.5)),
        "wo": _w((lambda *s: torch.randn(*s, dtype=torch.bfloat16) * 0.05)(Hq * Dh, D) * ((Hq * Dh) ** -0.5)),
    }

def reference(x, k_cache, v_cache, wq, wo):
    B, Sq, D = x.shape
    Hq, Dh = 32, 128
    Hkv = k_cache.shape[1]
    rep = Hq // Hkv
    q = (lambda x, *d: x.permute(*d))((x @ wq).reshape(B, Sq, Hq, Dh), 0, 2, 1, 3)
    k = (lambda x, n, axis: torch.repeat_interleave(x, n, dim=axis))(k_cache, rep, axis=1)
    v = (lambda x, n, axis: torch.repeat_interleave(x, n, dim=axis))(v_cache, rep, axis=1)
    scores = torch.einsum("bhqd,bhkd->bhqk", q, k) * (Dh ** -0.5)
    p = _softmax(scores, axis=-1)
    o = torch.einsum("bhqk,bhkd->bhqd", p, v)
    return (lambda x, *d: x.permute(*d))(o, 0, 2, 1, 3).reshape(B, Sq, Hq * Dh) @ wo
```

```cpp
#include <hip/hip_runtime.h>
#include <hip/hip_bf16.h>
#include <stdint.h>
#include <type_traits>

#define AS3 __attribute__((address_space(3)))
typedef __attribute__((ext_vector_type(8))) __bf16 bf16x8;
typedef __attribute__((ext_vector_type(2))) __bf16 bf16x2;
typedef __attribute__((ext_vector_type(4))) float f32x4;
typedef __attribute__((ext_vector_type(2))) float f32x2;
typedef __attribute__((ext_vector_type(4))) unsigned u32x4;
typedef __attribute__((ext_vector_type(2))) unsigned u32x2;
typedef __attribute__((ext_vector_type(4))) short i16x4;

namespace {

constexpr int SQ = 48, DM = 512, SK = 2048, DH = 128, NQ = 4096;

constexpr unsigned SLOT = 32768;
constexpr unsigned PB = 131072;
constexpr unsigned MISC = 155648;
constexpr unsigned MX_OFF = MISC;
constexpr unsigned LS_OFF = MISC + 1536;
constexpr unsigned FL_OFF = MISC + 3072;
constexpr unsigned XIMG = 98304;
constexpr unsigned QIMG = 98304;
constexpr unsigned LDS_BYTES = 163840;

constexpr size_t WS_EPOCH = 0;
constexpr size_t WS_IN1 = 4096;
constexpr size_t WS_IN2 = WS_IN1 + (size_t)256 * 32768;

constexpr float SCL = 0.08838834764831845f * 1.4426950408889634f;
constexpr float THR = 16.0f;
constexpr unsigned SPIN_CAP = 1u << 16;

__device__ __forceinline__ u32x4 lds_ld128(unsigned a) { return *(const AS3 u32x4*)(size_t)a; }
__device__ __forceinline__ void lds_st128(unsigned a, u32x4 v) { *(AS3 u32x4*)(size_t)a = v; }
__device__ __forceinline__ float lds_ldf(unsigned a) { return *(const AS3 float*)(size_t)a; }
__device__ __forceinline__ void lds_stf(unsigned a, float v) { *(AS3 float*)(size_t)a = v; }
__device__ __forceinline__ unsigned lds_ldu(unsigned a) { return *(const AS3 unsigned*)(size_t)a; }
__device__ __forceinline__ void lds_stu(unsigned a, unsigned v) { *(AS3 unsigned*)(size_t)a = v; }
__device__ __forceinline__ u32x2 lds_tr(unsigned a) {
  i16x4 r = __builtin_amdgcn_ds_read_tr16_b64_v4i16((AS3 i16x4*)(size_t)a);
  return __builtin_bit_cast(u32x2, r);
}
__device__ __forceinline__ f32x4 mfma(u32x4 a, u32x4 b, f32x4 c) {
  return __builtin_amdgcn_mfma_f32_16x16x32_bf16(__builtin_bit_cast(bf16x8, a), __builtin_bit_cast(bf16x8, b), c, 0, 0, 0);
}
__device__ __forceinline__ unsigned pk_bf16(float lo, float hi) {
  f32x2 v = {lo, hi};
  bf16x2 r = __builtin_convertvector(v, bf16x2);
  return __builtin_bit_cast(unsigned, r);
}
__device__ __forceinline__ float u2f(unsigned u) { return __builtin_bit_cast(float, u); }
__device__ __forceinline__ unsigned f2u(float f) { return __builtin_bit_cast(unsigned, f); }
__device__ __forceinline__ float bf_lo(unsigned u) { return __builtin_bit_cast(float, u << 16); }
__device__ __forceinline__ float bf_hi(unsigned u) { return __builtin_bit_cast(float, u & 0xffff0000u); }

template <unsigned LDS_IMM>
__device__ __forceinline__ void dma_nt(unsigned voff, const void* sbase, unsigned lds_s) {
  unsigned keep;
  asm volatile("s_mov_b32 %0, m0\n\ts_add_u32 m0, %3, %4\n\ts_nop 0\n\tglobal_load_lds_dwordx4 %1, %2 nt\n\ts_mov_b32 m0, %0"
               : "=&s"(keep) : "v"(voff), "s"(sbase), "s"(lds_s), "n"(LDS_IMM) : "memory", "scc");
}
template <unsigned LDS_IMM>
__device__ __forceinline__ void dma_def(unsigned voff, const void* sbase, unsigned lds_s) {
  unsigned keep;
  asm volatile("s_mov_b32 %0, m0\n\ts_add_u32 m0, %3, %4\n\ts_nop 0\n\tglobal_load_lds_dwordx4 %1, %2\n\ts_mov_b32 m0, %0"
               : "=&s"(keep) : "v"(voff), "s"(sbase), "s"(lds_s), "n"(LDS_IMM) : "memory", "scc");
}
template <int I, int N, class F>
__device__ __forceinline__ void static_for(F&& f) {
  if constexpr (I < N) {
    f(std::integral_constant<int, I>{});
    static_for<I + 1, N>(f);
  }
}
template <int N>
__device__ __forceinline__ void wait_barrier() {
  asm volatile("s_waitcnt vmcnt(%0) lgkmcnt(0)\n\ts_barrier" ::"n"(N) : "memory");
}
__device__ __forceinline__ void lds_barrier() { asm volatile("s_waitcnt lgkmcnt(0)\n\ts_barrier" ::: "memory"); }

__device__ __forceinline__ float grp_max(float v) {
  unsigned u = __builtin_bit_cast(unsigned, v);
  auto a = __builtin_amdgcn_permlane16_swap(u, u, false, false);
  v = fmaxf(u2f(a[0]), u2f(a[1]));
  u = __builtin_bit_cast(unsigned, v);
  auto c = __builtin_amdgcn_permlane32_swap(u, u, false, false);
  return fmaxf(u2f(c[0]), u2f(c[1]));
}
__device__ __forceinline__ float grp_sum(float v) {
  unsigned u = __builtin_bit_cast(unsigned, v);
  auto a = __builtin_amdgcn_permlane16_swap(u, u, false, false);
  v = u2f(a[0]) + u2f(a[1]);
  u = __builtin_bit_cast(unsigned, v);
  auto c = __builtin_amdgcn_permlane32_swap(u, u, false, false);
  return u2f(c[0]) + u2f(c[1]);
}
__device__ __forceinline__ float half_sum(float v) {
  unsigned u = __builtin_bit_cast(unsigned, v);
  auto c = __builtin_amdgcn_permlane32_swap(u, u, false, false);
  return u2f(c[0]) + u2f(c[1]);
}

__device__ __forceinline__ unsigned rotl5(unsigned h) { return __builtin_amdgcn_alignbit(h, h, 27); }
__device__ __forceinline__ void hash13(const u32x4& a, const u32x4& b, const u32x4& c, unsigned d, unsigned tag,
                                       unsigned& h1, unsigned& h2) {
  unsigned x = tag ^ 0x9E3779B9u, y = ~tag;
#pragma unroll
  for (int k = 0; k < 4; ++k) { x = (rotl5(x) ^ a[k]) + 0x85EBCA6Bu; }
#pragma unroll
  for (int k = 0; k < 2; ++k) { x = (rotl5(x) ^ b[k]) + 0x85EBCA6Bu; }
#pragma unroll
  for (int k = 2; k < 4; ++k) { y = (rotl5(y) ^ b[k]) + 0xC2B2AE35u; }
#pragma unroll
  for (int k = 0; k < 4; ++k) { y = (rotl5(y) ^ c[k]) + 0xC2B2AE35u; }
  y = (rotl5(y) ^ d) + 0xC2B2AE35u;
  h1 = (x ^ rotl5(y)) + y;
  h2 = (a[0] + a[1] + a[2]) + (a[3] + b[0] + b[1]) + (b[2] + b[3] + c[0]) + (c[1] + c[2] + c[3]) + (d + 2u * tag + 1u);
}

__global__ __launch_bounds__(512, 2) void gqa_fused_kernel(const uint16_t* __restrict__ x, const uint16_t* __restrict__ kc,
                                                           const uint16_t* __restrict__ vc, const uint16_t* __restrict__ wq,
                                                           const uint16_t* __restrict__ wo, uint16_t* __restrict__ out,
                                                           unsigned char* __restrict__ ws) {
  constexpr unsigned L0 = 0;
  const int tid = threadIdx.x;
  const int lane = tid & 63;
  const int w = __builtin_amdgcn_readfirstlane(tid >> 6);
  const int bid = blockIdx.x;
  const int kvh = bid & 7, slot = bid >> 3, hp = slot & 1, kh = (slot >> 1) & 1, b = slot >> 2;
  const int g = lane >> 4, i16 = lane & 15;
  const int hh = w >> 2, jq = w & 3;
  const int head0 = kvh * 4 + hp * 2;

  unsigned* epoch_ptr = (unsigned*)(ws + WS_EPOCH + (size_t)b * 256);
  const unsigned epoch_v = __hip_atomic_load(epoch_ptr, __ATOMIC_RELAXED, __HIP_MEMORY_SCOPE_AGENT);

  const char* kbase = (const char*)kc + ((size_t)(b * 8 + kvh) * SK + kh * 1024) * (DH * 2);
  const char* vbase = (const char*)vc + ((size_t)(b * 8 + kvh) * SK + kh * 1024) * (DH * 2);
  const char* wobase = (const char*)wo + ((size_t)head0 * DH * DM + kh * 256) * 2;

  const unsigned voffK = (unsigned)(w * 1024 + g * 256 + ((i16 ^ ((4 * (w & 3) + g) & 15)) << 4));
  const unsigned swzV = (unsigned)((i16 ^ (2 * ((4 * (w & 1) + g) & 7))) << 4);
  const unsigned voffV = (unsigned)(w * 1024 + g * 256) + swzV;
  const unsigned voffW = (unsigned)((4 * w + g) * 1024) + swzV;

  const unsigned w1k = (unsigned)(w * 1024);
  u32x4 qf[4][3];

  if (w < 4) {
    const char* xb = (const char*)x + (size_t)b * SQ * DM * 2;
    static_for<0, 12>([&](auto rc) {
      constexpr int r = decltype(rc)::value;
      const int row = w * 12 + r;
      dma_def<XIMG + r * 1024>((unsigned)((lane ^ (row & 15)) << 4), xb + row * 1024, (unsigned)(w * 12288));
    });
    static_for<0, 8>([&](auto ic) { constexpr int i = decltype(ic)::value; dma_nt<0 * SLOT + i * 4096>(voffK, kbase + i * 4096, w1k); });
    static_for<0, 8>([&](auto ic) { constexpr int i = decltype(ic)::value; dma_nt<1 * SLOT + i * 4096>(voffK, kbase + 32768 + i * 4096, w1k); });
    static_for<0, 8>([&](auto ic) { constexpr int i = decltype(ic)::value; dma_nt<2 * SLOT + i * 4096>(voffV, vbase + i * 4096, w1k); });
    if (lane < 16) lds_stu(L0 + FL_OFF + lane * 4, 0u);
    wait_barrier<24>();
    lds_barrier();
    lds_barrier();
  } else {
    const int idx = w - 4, qh = idx >> 1, half = idx & 1;
    const __amdgpu_buffer_rsrc_t wq_rs = __builtin_amdgcn_make_buffer_rsrc((void*)wq, 0, DM * NQ * 2, 0x00020000);
    const unsigned wq_voff = (unsigned)((8 * g) * (NQ * 2) + ((head0 + qh) * DH + 64 * half + 4 * i16) * 2);
    constexpr int PRE = 6;
    u32x2 wr[16][8];
#pragma unroll
    for (int ks = 0; ks < PRE; ++ks)
#pragma unroll
      for (int j = 0; j < 8; ++j)
        wr[ks][j] = __builtin_bit_cast(u32x2, __builtin_amdgcn_raw_buffer_load_b64(wq_rs, wq_voff, (32 * ks + j) * (NQ * 2), 0));
    wait_barrier<63>();
    f32x4 acc[4][3];
#pragma unroll
    for (int e = 0; e < 4; ++e)
#pragma unroll
      for (int t = 0; t < 3; ++t) acc[e][t] = f32x4{0.f, 0.f, 0.f, 0.f};
    unsigned xa[4];
#pragma unroll
    for (int k = 0; k < 4; ++k) xa[k] = L0 + XIMG + i16 * 1024 + (((4 * k + g) ^ i16) << 4);
#pragma unroll
    for (int ks = 0; ks < 16; ++ks) {
      if (ks + PRE < 16) {
#pragma unroll
        for (int j = 0; j < 8; ++j)
          wr[ks + PRE][j] = __builtin_bit_cast(u32x2, __builtin_amdgcn_raw_buffer_load_b64(wq_rs, wq_voff, (32 * (ks + PRE) + j) * (NQ * 2), 0));
      }
      u32x4 xf[3];
#pragma unroll
      for (int t = 0; t < 3; ++t) xf[t] = lds_ld128(xa[ks & 3] + (ks >> 2) * 256 + t * 16384);
#pragma unroll
      for (int e = 0; e < 4; ++e) {
        u32x4 af;
#pragma unroll
        for (int r = 0; r < 4; ++r)
          af[r] = __builtin_amdgcn_perm(wr[ks][2 * r + 1][e >> 1], wr[ks][2 * r][e >> 1], (e & 1) ? 0x07060302u : 0x05040100u);
#pragma unroll
        for (int t = 0; t < 3; ++t) acc[e][t] = mfma(af, xf[t], acc[e][t]);
      }
      __builtin_amdgcn_sched_barrier(0);
    }
    lds_barrier();
#pragma unroll
    for (int u = 0; u < 2; ++u)
#pragma unroll
      for (int t = 0; t < 3; ++t) {
        u32x4 f;
        f[0] = pk_bf16(acc[0][t][2 * u], acc[1][t][2 * u]);
        f[1] = pk_bf16(acc[2][t][2 * u], acc[3][t][2 * u]);
        f[2] = pk_bf16(acc[0][t][2 * u + 1], acc[1][t][2 * u + 1]);
        f[3] = pk_bf16(acc[2][t][2 * u + 1], acc[3][t][2 * u + 1]);
        lds_st128(L0 + QIMG + qh * 12288 + (2 * half + u) * 3072 + t * 1024 + lane * 16, f);
      }
    lds_barrier();
  }
#pragma unroll
  for (int s = 0; s < 4; ++s)
#pragma unroll
    for (int t = 0; t < 3; ++t) qf[s][t] = lds_ld128(L0 + QIMG + hh * 12288 + s * 3072 + t * 1024 + lane * 16);

  unsigned ka[4];
#pragma unroll
  for (int s = 0; s < 4; ++s) ka[s] = L0 + (32 * jq + i16) * 256 + (((8 * (s >> 1) + 2 * g + (s & 1)) ^ i16) << 4);
  unsigned va[2];
  {
    const int q_ = (lane >> 2) & 3, p = lane & 3;
#pragma unroll
    for (int dt = 0; dt < 2; ++dt)
      va[dt] = L0 + (4 * g + q_) * 256 + (((4 * jq + 2 * dt + (p >> 1)) ^ (2 * ((4 * g + q_) & 7))) << 4) + 8 * (p & 1);
  }
  const unsigned pw = L0 + PB + hh * 12288 + jq * 3072 + lane * 16;
  const unsigned pr = L0 + PB + lane * 16;

  f32x4 O[2][3];
#pragma unroll
  for (int dt = 0; dt < 2; ++dt)
#pragma unroll
    for (int t = 0; t < 3; ++t) O[dt][t] = f32x4{0.f, 0.f, 0.f, 0.f};
  float lsum[3] = {0.f, 0.f, 0.f};
  float mref[3] = {-INFINITY, -INFINITY, -INFINITY};
  float thr_raw[3] = {-INFINITY, -INFINITY, -INFINITY};

  auto qk = [&](unsigned soff, f32x4(&S)[2][3]) {
#pragma unroll
    for (int kt = 0; kt < 2; ++kt) {
#pragma unroll
      for (int s = 0; s < 4; ++s) {
        const u32x4 kf = lds_ld128(ka[s] + soff + kt * 4096);
#pragma unroll
        for (int t = 0; t < 3; ++t) S[kt][t] = mfma(kf, qf[s][t], s == 0 ? f32x4{0.f, 0.f, 0.f, 0.f} : S[kt][t]);
      }
    }
  };
  auto pv = [&](unsigned soff, unsigned poff, f32x4(&A)[2][3]) {
#pragma unroll
    for (int ks = 0; ks < 4; ++ks) {
      u32x4 pf[3];
#pragma unroll
      for (int t = 0; t < 3; ++t) pf[t] = lds_ld128(pr + poff + ks * 3072 + t * 1024);
#pragma unroll
      for (int dt = 0; dt < 2; ++dt) {
        const u32x2 v0 = lds_tr(va[dt] + soff + ks * 8192);
        const u32x2 v1 = lds_tr(va[dt] + soff + ks * 8192 + 4096);
        const u32x4 vf = {v0[0], v0[1], v1[0], v1[1]};
#pragma unroll
        for (int t = 0; t < 3; ++t) A[dt][t] = mfma(vf, pf[t], A[dt][t]);
      }
    }
  };
  auto tile_max = [&](const f32x4(&S)[2][3], int t) {
    float m = fmaxf(fmaxf(S[0][t][0], S[0][t][1]), S[0][t][2]);
    m = fmaxf(fmaxf(m, S[0][t][3]), S[1][t][0]);
    m = fmaxf(fmaxf(m, S[1][t][1]), S[1][t][2]);
    return fmaxf(m, S[1][t][3]);
  };
  auto remax = [&](const f32x4(&S)[2][3]) {
#pragma unroll
    for (int t = 0; t < 3; ++t) {
      const float m = grp_max(tile_max(S, t));
      if (lane < 16) lds_stf(L0 + MX_OFF + ((hh * 4 + jq) * 3 + t) * 64 + lane * 4, m);
    }
    lds_barrier();
#pragma unroll
    for (int t = 0; t < 3; ++t) {
      float m = lds_ldf(L0 + MX_OFF + ((hh * 4 + 0) * 3 + t) * 64 + i16 * 4);
#pragma unroll
      for (int j = 1; j < 4; ++j) m = fmaxf(m, lds_ldf(L0 + MX_OFF + ((hh * 4 + j) * 3 + t) * 64 + i16 * 4));
      const float mn = fmaxf(m * SCL, mref[t]);
      const float alpha = __builtin_amdgcn_exp2f(mref[t] - mn);
      lsum[t] *= alpha;
#pragma unroll
      for (int dt = 0; dt < 2; ++dt) O[dt][t] *= alpha;
      mref[t] = mn;
      thr_raw[t] = (mn + THR) * (1.0f / SCL);
    }
  };
  auto softmax_store = [&](const f32x4(&S)[2][3]) {
#pragma unroll
    for (int t = 0; t < 3; ++t) {
      float p[2][4];
#pragma unroll
      for (int kt = 0; kt < 2; ++kt)
#pragma unroll
        for (int r = 0; r < 4; ++r) {
          p[kt][r] = __builtin_amdgcn_exp2f(__builtin_fmaf(S[kt][t][r], SCL, -mref[t]));
          lsum[t] += p[kt][r];
        }
      u32x4 f = {pk_bf16(p[0][0], p[0][1]), pk_bf16(p[0][2], p[0][3]), pk_bf16(p[1][0], p[1][1]), pk_bf16(p[1][2], p[1][3])};
      lds_st128(pw + t * 1024, f);
    }
  };
  auto check = [&](const f32x4(&S)[2][3], int tn) {
    bool over = false;
#pragma unroll
    for (int t = 0; t < 3; ++t) over |= tile_max(S, t) > thr_raw[t];
    if (__builtin_amdgcn_ballot_w64(over) != 0ull) {
      if (lane == 0) lds_stu(L0 + FL_OFF + tn * 4, 1u);
    }
  };
  unsigned w1m = w1k;
  asm volatile("" : "+s"(w1m));
  auto req_kv = [&](const char* src, unsigned voff, auto slc) {
    constexpr unsigned sl = decltype(slc)::value;
    static_for<0, 4>([&](auto ic) { constexpr int i = decltype(ic)::value; dma_nt<sl * SLOT + i * 8192>(voff, src + i * 8192, w1m); });
  };
  auto req_w = [&](auto wic) {
    constexpr int wi = decltype(wic)::value;
    const char* src = wobase + (size_t)(wi >> 1) * 128 * 1024 + (wi & 1) * 256;
    static_for<0, 4>([&](auto ic) { constexpr int i = decltype(ic)::value; dma_def<wi * SLOT + i * 8192>(voffW, src + i * 32768, w1m); });
  };
#define IC(v) std::integral_constant<int, (v)>{}

  f32x4 SA[2][3], SB[2][3];
  wait_barrier<16>();
  req_kv(kbase + 2 * 32768, voffK, IC(3));
  qk(0 * SLOT, SA);
  unsigned flag = 1;

#define SUPER(PAR, t, SC, SN)                                                                       \
  {                                                                                                 \
                                   \
    wait_barrier<8>();                                                                              \
    if ((t) <= 5) req_kv(vbase + (size_t)((t) + 1) * 32768, voffV, IC((PAR) ? 2 : 0));                  \
    else req_w(IC((PAR) ? 2 : 0));                                                                   \
    if (flag) remax(SC);                                                                            \
    if ((t) < 7) qk(((PAR) ? 3 : 1) * SLOT, SN);                                                    \
    softmax_store(SC);                                                                              \
    if ((t) < 7) check(SN, (t) + 1);                                                                \
    else {                                                                                          \
      _Pragma("unroll") for (int tt = 0; tt < 3; ++tt) {                                            \
        const float s_ = grp_sum(lsum[tt]);                                                         \
        if (lane < 16) lds_stf(L0 + LS_OFF + ((hh * 4 + jq) * 3 + tt) * 64 + lane * 4, s_);         \
      }                                                                                             \
    }                                                                                               \
                 \
    if ((t) < 7) wait_barrier<8>(); else wait_barrier<12>();                                        \
    if ((t) <= 4) req_kv(kbase + (size_t)((t) + 3) * 32768, voffK, IC((PAR) ? 3 : 1));                  \
    else if ((t) == 5) req_kv(vbase + (size_t)7 * 32768, voffV, IC(3));                                 \
    else if ((t) == 6) req_w(IC(1));                                                                    \
    flag = __builtin_amdgcn_readfirstlane(lds_ldu(L0 + FL_OFF + (((t) + 1) & 15) * 4));             \
    pv(((t) == 7 ? 3 : ((PAR) ? 0 : 2)) * SLOT, hh * 12288, O);                                     \
  }

#pragma unroll 1
  for (int t2 = 0; t2 < 8; t2 += 2) {
    SUPER(0, t2, SA, SB)
    SUPER(1, t2 + 1, SB, SA)
  }
#undef SUPER

  float lt[3];
#pragma unroll
  for (int t = 0; t < 3; ++t) {
    float s = 0.f;
#pragma unroll
    for (int j = 0; j < 4; ++j) s += lds_ldf(L0 + LS_OFF + ((hh * 4 + j) * 3 + t) * 64 + i16 * 4);
    lt[t] = s;
  }
  lds_barrier();
  req_w(IC(3));

  const unsigned tag = (unsigned)__builtin_amdgcn_readfirstlane(epoch_v) + 1u;
  const __amdgpu_buffer_rsrc_t ws_rs = __builtin_amdgcn_make_buffer_rsrc((void*)ws, 0, (unsigned)(WS_IN2 + (size_t)256 * 32768), 0x00020000);

  float lam[3];
  u32x4 mine[3];
#pragma unroll
  for (int t = 0; t < 3; ++t) {
    const float inv = 1.0f / lt[t];
    lam[t] = mref[t] + __builtin_amdgcn_logf(lt[t]);
#pragma unroll
    for (int dt = 0; dt < 2; ++dt) O[dt][t] *= inv;
    mine[t] = u32x4{pk_bf16(O[0][t][0], O[0][t][1]), pk_bf16(O[0][t][2], O[0][t][3]), pk_bf16(O[1][t][0], O[1][t][1]),
                    pk_bf16(O[1][t][2], O[1][t][3])};
  }
  {
    const float l0 = lam[0], l1 = lam[1], l2 = lam[2];
    const unsigned lw = __builtin_bit_cast(unsigned, (lane & 32) ? l2 : ((lane & 16) ? l1 : l0));
    unsigned h1, h2;
    hash13(mine[0], mine[1], mine[2], lw, tag, h1, h2);
    const unsigned dst = (unsigned)(WS_IN1 + (size_t)(bid ^ 16) * 32768 + w * 4096 + lane * 16);
    __builtin_amdgcn_raw_buffer_store_b128(mine[0], ws_rs, dst, 0, 16);
    __builtin_amdgcn_raw_buffer_store_b128(mine[1], ws_rs, dst + 1024, 0, 16);
    __builtin_amdgcn_raw_buffer_store_b128(mine[2], ws_rs, dst + 2048, 0, 16);
    __builtin_amdgcn_raw_buffer_store_b128(u32x4{lw, h1, h2, tag}, ws_rs, dst + 3072, 0, 16);
  }
  u32x4 th[4];
  {
    const unsigned src = (unsigned)(WS_IN1 + (size_t)bid * 32768 + w * 4096 + lane * 16);
    for (unsigned spin = 0; spin < SPIN_CAP; ++spin) {
      asm volatile("" ::: "memory");
#pragma unroll
      for (int k = 0; k < 4; ++k) th[k] = __builtin_amdgcn_raw_buffer_load_b128(ws_rs, src + k * 1024, 0, 16);
      unsigned h1, h2;
      hash13(th[0], th[1], th[2], th[3][0], tag, h1, h2);
      const bool ok = (h1 == th[3][1]) & (h2 == th[3][2]);
      if (__builtin_amdgcn_ballot_w64(ok) == ~0ull) break;
      __builtin_amdgcn_s_sleep(1);
    }
  }
  {
    const unsigned u = th[3][0];
    auto a = __builtin_amdgcn_permlane32_swap(u, u, false, false);
    auto c0 = __builtin_amdgcn_permlane16_swap(a[0], a[0], false, false);
    auto c1 = __builtin_amdgcn_permlane16_swap(a[1], a[1], false, false);
    const float lb[3] = {u2f(c0[0]), u2f(c0[1]), u2f(c1[0])};
#pragma unroll
    for (int t = 0; t < 3; ++t) {
      const float mx = fmaxf(lam[t], lb[t]);
      float wa = __builtin_amdgcn_exp2f(lam[t] - mx), wb = __builtin_amdgcn_exp2f(lb[t] - mx);
      const float inv = 1.0f / (wa + wb);
      wa *= inv;
      wb *= inv;
      float m[8];
#pragma unroll
      for (int k = 0; k < 4; ++k) {
        m[2 * k] = wa * O[k >> 1][t][2 * (k & 1)] + wb * bf_lo(th[t][k]);
        m[2 * k + 1] = wa * O[k >> 1][t][2 * (k & 1) + 1] + wb * bf_hi(th[t][k]);
      }
      lds_st128(pw + t * 1024, u32x4{pk_bf16(m[0], m[1]), pk_bf16(m[2], m[3]), pk_bf16(m[4], m[5]), pk_bf16(m[6], m[7])});
    }
  }
  wait_barrier<0>();

  f32x4 A[2][3];
#pragma unroll
  for (int dt = 0; dt < 2; ++dt)
#pragma unroll
    for (int t = 0; t < 3; ++t) A[dt][t] = f32x4{0.f, 0.f, 0.f, 0.f};
  pv((0 + hh) * SLOT, 0, A);
  pv((2 + hh) * SLOT, 12288, A);
  lds_barrier();

  {
    u32x4 f[3];
#pragma unroll
    for (int t = 0; t < 3; ++t)
      f[t] = u32x4{pk_bf16(A[0][t][0], A[0][t][1]), pk_bf16(A[0][t][2], A[0][t][3]), pk_bf16(A[1][t][0], A[1][t][1]),
                   pk_bf16(A[1][t][2], A[1][t][3])};
    unsigned h1, h2;
    hash13(f[0], f[1], f[2], 0u, tag, h1, h2);
    const int rho = 2 * w + (i16 >> 3);
    const int rbid = (rho >> 1) + 8 * ((rho & 1) + 2 * kh + 4 * b);
    const int pidx = kvh * 2 + hp;
    const unsigned dst = (unsigned)(WS_IN2 + (size_t)rbid * 32768 + pidx * 2048 + (8 * g + (i16 & 7)) * 16);
    __builtin_amdgcn_raw_buffer_store_b128(f[0], ws_rs, dst, 0, 16);
    __builtin_amdgcn_raw_buffer_store_b128(f[1], ws_rs, dst + 512, 0, 16);
    __builtin_amdgcn_raw_buffer_store_b128(f[2], ws_rs, dst + 1024, 0, 16);
    __builtin_amdgcn_raw_buffer_store_b128(u32x4{0u, h1, h2, tag}, ws_rs, dst + 1536, 0, 16);
  }
  {
    const unsigned src = (unsigned)(WS_IN2 + (size_t)bid * 32768 + (tid >> 5) * 2048 + (tid & 31) * 16);
    for (unsigned spin = 0; spin < SPIN_CAP; ++spin) {
      asm volatile("" ::: "memory");
#pragma unroll
      for (int k = 0; k < 4; ++k) th[k] = __builtin_amdgcn_raw_buffer_load_b128(ws_rs, src + k * 512, 0, 16);
      unsigned h1, h2;
      hash13(th[0], th[1], th[2], th[3][0], tag, h1, h2);
      const bool ok = (h1 == th[3][1]) & (h2 == th[3][2]);
      if (__builtin_amdgcn_ballot_w64(ok) == ~0ull) break;
      __builtin_amdgcn_s_sleep(1);
    }
#pragma unroll
    for (int t = 0; t < 3; ++t) {
      f32x4 r;
#pragma unroll
      for (int k = 0; k < 2; ++k) {
        const float a0 = half_sum(bf_lo(th[t][k])), a1 = half_sum(bf_hi(th[t][k]));
        const float b0 = half_sum(bf_lo(th[t][2 + k])), b1 = half_sum(bf_hi(th[t][2 + k]));
        r[2 * k] = lane < 32 ? a0 : b0;
        r[2 * k + 1] = lane < 32 ? a1 : b1;
      }
      lds_st128(L0 + PB + w * 3072 + t * 1024 + lane * 16, __builtin_bit_cast(u32x4, r));
    }
  }
  lds_barrier();
  if (tid < 192) {
    const int t = tid >> 6;
    f32x4 s = __builtin_bit_cast(f32x4, lds_ld128(L0 + PB + t * 1024 + lane * 16));
#pragma unroll
    for (int v = 1; v < 8; ++v) s += __builtin_bit_cast(f32x4, lds_ld128(L0 + PB + v * 3072 + t * 1024 + lane * 16));
    const int rho = kvh * 2 + hp;
    const int nt = lane >> 5, l32 = lane & 31, gg = l32 >> 3, q = 8 * (rho & 1) + (l32 & 7);
    const int col = 256 * kh + 32 * (rho >> 1) + 16 * nt + 4 * gg;
    u32x2 o = {pk_bf16(s[0], s[1]), pk_bf16(s[2], s[3])};
    *(u32x2*)(out + ((size_t)b * SQ + 16 * t + q) * DM + col) = o;
  }
  if (tid == 0 && kvh == 0 && hp == 0 && kh == 0) __hip_atomic_store(epoch_ptr, tag, __ATOMIC_RELAXED, __HIP_MEMORY_SCOPE_AGENT);
}

}

extern "C" void kernel_launch(void* const* d_in, const int* in_sizes, int n_in, void* d_out, int out_size, void* d_ws,
                              size_t ws_size, hipStream_t stream) {
  (void)in_sizes; (void)n_in; (void)out_size; (void)ws_size;
  (void)hipFuncSetAttribute((const void*)gqa_fused_kernel, hipFuncAttributeMaxDynamicSharedMemorySize, LDS_BYTES);
  gqa_fused_kernel<<<256, 512, LDS_BYTES, stream>>>((const uint16_t*)d_in[0], (const uint16_t*)d_in[1], (const uint16_t*)d_in[2],
                                                    (const uint16_t*)d_in[3], (const uint16_t*)d_in[4], (uint16_t*)d_out,
                                                    (unsigned char*)d_ws);
}
```

```cpp
#include <hip/hip_runtime.h>
#include <hip/hip_bf16.h>
#include <stdint.h>
#include <type_traits>

#define AS3 __attribute__((address_space(3)))
typedef __attribute__((ext_vector_type(8))) __bf16 bf16x8;
typedef __attribute__((ext_vector_type(2))) __bf16 bf16x2;
typedef __attribute__((ext_vector_type(4))) float f32x4;
typedef __attribute__((ext_vector_type(2))) float f32x2;
typedef __attribute__((ext_vector_type(4))) unsigned u32x4;
typedef __attribute__((ext_vector_type(2))) unsigned u32x2;
typedef __attribute__((ext_vector_type(4))) short i16x4;

#define STAMP(i) do {} while (0)
#define STAMPR(i) do {} while (0)
#define STAMP_DUMP() do { __builtin_amdgcn_s_waitcnt(0xc07f);} while (0)
#define SB() __builtin_amdgcn_sched_barrier(0)
#define LDMA(x) x
#define DSTAMP(i) do {} while (0)

namespace {

constexpr int SQ = 48, DM = 512, SK = 2048, DH = 128, NQ = 4096;

constexpr unsigned SLOT = 32768;
constexpr unsigned PB = 131072;
constexpr unsigned MISC = 155648;
constexpr unsigned MX_OFF = MISC;
constexpr unsigned LS_OFF = MISC + 1536;
constexpr unsigned FL_OFF = MISC + 3072;
constexpr unsigned XIMG = 98304;
constexpr unsigned LDS_BYTES = 163840;

constexpr size_t WS_EPOCH = 0;
constexpr size_t WS_IN1 = 4096;
constexpr size_t WS_IN2 = WS_IN1 + (size_t)256 * 32768;

constexpr float SCL = 0.08838834764831845f * 1.4426950408889634f;
constexpr unsigned SUM_CAP = 0x47800000u;
constexpr unsigned SPIN_CAP = 1u << 16;

__device__ __forceinline__ u32x4 lds_ld128(unsigned a) { return *(const AS3 u32x4*)(size_t)a; }
__device__ __forceinline__ void lds_st128(unsigned a, u32x4 v) { *(AS3 u32x4*)(size_t)a = v; }
__device__ __forceinline__ float lds_ldf(unsigned a) { return *(const AS3 float*)(size_t)a; }
__device__ __forceinline__ void lds_stf(unsigned a, float v) { *(AS3 float*)(size_t)a = v; }
__device__ __forceinline__ unsigned lds_ldu(unsigned a) { return *(const AS3 unsigned*)(size_t)a; }
__device__ __forceinline__ void lds_stu(unsigned a, unsigned v) { *(AS3 unsigned*)(size_t)a = v; }
__device__ __forceinline__ u32x2 lds_tr(unsigned a) {
  i16x4 r = __builtin_amdgcn_ds_read_tr16_b64_v4i16((AS3 i16x4*)(size_t)a);
  return __builtin_bit_cast(u32x2, r);
}
__device__ __forceinline__ f32x4 mfma(u32x4 a, u32x4 b, f32x4 c) {
  return __builtin_amdgcn_mfma_f32_16x16x32_bf16(__builtin_bit_cast(bf16x8, a), __builtin_bit_cast(bf16x8, b), c, 0, 0, 0);
}
__device__ __forceinline__ unsigned pk_bf16(float lo, float hi) {
  f32x2 v = {lo, hi};
  bf16x2 r = __builtin_convertvector(v, bf16x2);
  return __builtin_bit_cast(unsigned, r);
}
__device__ __forceinline__ float u2f(unsigned u) { return __builtin_bit_cast(float, u); }
__device__ __forceinline__ unsigned f2u(float f) { return __builtin_bit_cast(unsigned, f); }
__device__ __forceinline__ float bf_lo(unsigned u) { return __builtin_bit_cast(float, u << 16); }
__device__ __forceinline__ float bf_hi(unsigned u) { return __builtin_bit_cast(float, u & 0xffff0000u); }
__device__ __forceinline__ float ex2(float v) { return __builtin_amdgcn_exp2f(v); }

template <int OFF>
__device__ __forceinline__ void dma_nt(unsigned voff, const void* sbase, unsigned m0v) {
  asm volatile("s_mov_b32 m0, %2\n\ts_nop 0\n\tglobal_load_lds_dwordx4 %0, %1 offset:%3 nt"
               :: "v"(voff), "s"(sbase), "s"(m0v), "n"(OFF) : "memory");
}
template <int OFF>
__device__ __forceinline__ void dma_def(unsigned voff, const void* sbase, unsigned m0v) {
  asm volatile("s_mov_b32 m0, %2\n\ts_nop 0\n\tglobal_load_lds_dwordx4 %0, %1 offset:%3"
               :: "v"(voff), "s"(sbase), "s"(m0v), "n"(OFF) : "memory");
}
template <int I, int N, class F>
__device__ __forceinline__ void static_for(F&& f) {
  if constexpr (I < N) {
    f(std::integral_constant<int, I>{});
    static_for<I + 1, N>(f);
  }
}
template <int N>
__device__ __forceinline__ void wait_barrier() {
  asm volatile("s_waitcnt vmcnt(%0) lgkmcnt(0)\n\ts_barrier" ::"n"(N) : "memory");
}
__device__ __forceinline__ void lds_barrier() { asm volatile("s_waitcnt lgkmcnt(0)\n\ts_barrier" ::: "memory"); }

__device__ __forceinline__ float grp_max(float v) {
  unsigned u = __builtin_bit_cast(unsigned, v);
  auto a = __builtin_amdgcn_permlane16_swap(u, u, false, false);
  v = fmaxf(u2f(a[0]), u2f(a[1]));
  u = __builtin_bit_cast(unsigned, v);
  auto c = __builtin_amdgcn_permlane32_swap(u, u, false, false);
  return fmaxf(u2f(c[0]), u2f(c[1]));
}
__device__ __forceinline__ float half_sum(float v) {
  unsigned u = __builtin_bit_cast(unsigned, v);
  auto c = __builtin_amdgcn_permlane32_swap(u, u, false, false);
  return u2f(c[0]) + u2f(c[1]);
}

__device__ __forceinline__ unsigned rotl5(unsigned h) { return __builtin_amdgcn_alignbit(h, h, 27); }
__device__ __forceinline__ void hash13(const u32x4& a, const u32x4& b, const u32x4& c, unsigned d, unsigned tag,
                                       unsigned& h1, unsigned& h2) {
  unsigned x = tag ^ 0x9E3779B9u, y = ~tag;
#pragma unroll
  for (int k = 0; k < 4; ++k) { x = (rotl5(x) ^ a[k]) + 0x85EBCA6Bu; }
#pragma unroll
  for (int k = 0; k < 2; ++k) { x = (rotl5(x) ^ b[k]) + 0x85EBCA6Bu; }
#pragma unroll
  for (int k = 2; k < 4; ++k) { y = (rotl5(y) ^ b[k]) + 0xC2B2AE35u; }
#pragma unroll
  for (int k = 0; k < 4; ++k) { y = (rotl5(y) ^ c[k]) + 0xC2B2AE35u; }
  y = (rotl5(y) ^ d) + 0xC2B2AE35u;
  h1 = (x ^ rotl5(y)) + y;
  h2 = (a[0] + a[1] + a[2]) + (a[3] + b[0] + b[1]) + (b[2] + b[3] + c[0]) + (c[1] + c[2] + c[3]) + (d + 2u * tag + 1u);
}

template <int STR>
__device__ __forceinline__ void recv4(const __amdgpu_buffer_rsrc_t& rs, unsigned src, unsigned tag, u32x4 (&th)[4]) {
  for (unsigned spin = 0; spin < 64; ++spin) {
    asm volatile("" ::: "memory");
#pragma unroll
    for (int k = 0; k < 4; ++k) th[k] = __builtin_amdgcn_raw_buffer_load_b128(rs, src + k * STR, 0, 16);
    unsigned h1, h2;
    hash13(th[0], th[1], th[2], th[3][0], tag, h1, h2);
    const bool ok = (h1 == th[3][1]) & (h2 == th[3][2]);
    if (__builtin_amdgcn_ballot_w64(ok) == ~0ull) break;
    for (unsigned s2 = 0; s2 < SPIN_CAP; ++s2) {
      asm volatile("" ::: "memory");
      const unsigned tg = __builtin_amdgcn_raw_buffer_load_b32(rs, src + 3 * STR + 12, 0, 16);
      if (__builtin_amdgcn_ballot_w64(tg == tag) == ~0ull) break;
      __builtin_amdgcn_s_sleep(2);
    }
  }
}

__global__ __launch_bounds__(512, 2) void gqa_fused_kernel(const uint16_t* __restrict__ x, const uint16_t* __restrict__ kc,
                                                           const uint16_t* __restrict__ vc, const uint16_t* __restrict__ wq,
                                                           const uint16_t* __restrict__ wo, uint16_t* __restrict__ out,
                                                           unsigned char* __restrict__ ws) {
  const int tid = threadIdx.x;
  const int lane = tid & 63;
  const int w = __builtin_amdgcn_readfirstlane(tid >> 6);
  const int bid = blockIdx.x;
  const int kvh = bid & 7, slot = bid >> 3, hp = slot & 1, kh = (slot >> 1) & 1, b = slot >> 2;
  const int g = lane >> 4, i16 = lane & 15;
  const int hh = w >> 2, jq = w & 3;
  const int head0 = kvh * 4 + hp * 2;

  STAMPR(0);
  unsigned* epoch_ptr = (unsigned*)(ws + WS_EPOCH + (size_t)b * 256);
  const unsigned epoch_v = __hip_atomic_load(epoch_ptr, __ATOMIC_RELAXED, __HIP_MEMORY_SCOPE_AGENT);

  const char* kbase = (const char*)kc + ((size_t)(b * 8 + kvh) * SK + kh * 1024) * (DH * 2);
  const char* vbase = (const char*)vc + ((size_t)(b * 8 + kvh) * SK + kh * 1024) * (DH * 2);
  const char* wobase = (const char*)wo + ((size_t)head0 * DH * DM + kh * 256) * 2;

  unsigned voffK[4], voffV[2];
#pragma unroll
  for (int i = 0; i < 4; ++i) voffK[i] = (unsigned)(w * 4096 + g * 256 + ((i16 ^ (4 * i + g)) << 4));
#pragma unroll
  for (int i = 0; i < 2; ++i) voffV[i] = (unsigned)(w * 4096 + g * 256 + ((i16 ^ (2 * (4 * i + g))) << 4));
  const unsigned w4k = (unsigned)(w * 4096);
  u32x4 qf[4][3];

  if (w < 4) {
    const char* xb = (const char*)x + (size_t)b * SQ * DM * 2;
    static_for<0, 12>([&](auto rc) {
      constexpr int r = decltype(rc)::value;
      const int row = w * 12 + r;
      dma_def<0>((unsigned)((lane ^ (row & 15)) << 4), xb + row * 1024, (unsigned)(XIMG + row * 1024));
    });
    auto item = [&](auto n0c, auto n1c) {
      static_for<decltype(n0c)::value, decltype(n1c)::value>([&](auto nc) {
        constexpr int n = decltype(nc)::value, it = n >> 3, vw = (n >> 2) & 1, i = n & 3;
        const char* src = (it == 2 ? vbase : kbase + it * 32768) + vw * 16384;
        dma_nt<i * 1024>(it == 2 ? voffV[i & 1] : voffK[i], src, w4k + it * SLOT + vw * 16384);
        __builtin_amdgcn_s_sleep(2);
      });
    };
    item(std::integral_constant<int, 0>{}, std::integral_constant<int, 6>{});
    if (lane < 16) lds_stu(FL_OFF + lane * 4, 0u);
    wait_barrier<6>();
    item(std::integral_constant<int, 6>{}, std::integral_constant<int, 24>{});
    lds_barrier();
    lds_barrier();
    lds_barrier();
  } else {
    const int idx = w - 4, qh = idx >> 1, khalf = idx & 1;
    const __amdgpu_buffer_rsrc_t wq_rs = __builtin_amdgcn_make_buffer_rsrc((void*)wq, 0, DM * NQ * 2, 0x00020000);
    const unsigned wq_voff = (unsigned)((256 * khalf + 8 * g) * (NQ * 2) + (head0 + qh) * (DH * 2) + i16 * 16);
    constexpr int PRE = 3;
    u32x4 wr[8][8];
#pragma unroll
    for (int ks = 0; ks < PRE; ++ks)
#pragma unroll
      for (int j = 0; j < 8; ++j) wr[ks][j] = __builtin_amdgcn_raw_buffer_load_b128(wq_rs, wq_voff, (32 * ks + j) * (NQ * 2), 0);
    wait_barrier<63>();
    STAMP(29);
    f32x4 acc[8][3];
#pragma unroll
    for (int e = 0; e < 8; ++e)
#pragma unroll
      for (int t = 0; t < 3; ++t) acc[e][t] = f32x4{0.f, 0.f, 0.f, 0.f};
    unsigned xa[4];
#pragma unroll
    for (int k = 0; k < 4; ++k) xa[k] = XIMG + i16 * 1024 + khalf * 512 + (((4 * k + g) ^ i16) << 4);
#pragma unroll
    for (int ks = 0; ks < 8; ++ks) {
      u32x4 xf[3];
#pragma unroll
      for (int t = 0; t < 3; ++t) xf[t] = lds_ld128(xa[ks & 3] + (ks >> 2) * 256 + t * 16384);
#pragma unroll
      for (int e = 0; e < 8; ++e) {
        u32x4 af;
#pragma unroll
        for (int r = 0; r < 4; ++r)
          af[r] = __builtin_amdgcn_perm(wr[ks][2 * r + 1][e >> 1], wr[ks][2 * r][e >> 1], (e & 1) ? 0x07060302u : 0x05040100u);
#pragma unroll
        for (int t = 0; t < 3; ++t) acc[e][t] = mfma(af, xf[t], acc[e][t]);
      }
      SB();
      if (ks + PRE < 8) {
#pragma unroll
        for (int j = 0; j < 8; ++j)
          wr[ks + PRE][j] = __builtin_amdgcn_raw_buffer_load_b128(wq_rs, wq_voff, (32 * (ks + PRE) + j) * (NQ * 2), 0);
      }
      SB();
    }
    STAMP(30);
    lds_barrier();
    {
      const unsigned snd = XIMG + (idx ^ 1) * 12288 + lane * 16;
#pragma unroll
      for (int t = 0; t < 3; ++t)
#pragma unroll
        for (int rr = 0; rr < 2; ++rr) {
          f32x4 lo, hi;
#pragma unroll
          for (int e = 0; e < 4; ++e) {
            lo[e] = khalf ? acc[e][t][rr] : acc[e][t][2 + rr];
            hi[e] = khalf ? acc[4 + e][t][rr] : acc[4 + e][t][2 + rr];
          }
          lds_st128(snd + (t * 2 + rr) * 2048, __builtin_bit_cast(u32x4, lo));
          lds_st128(snd + (t * 2 + rr) * 2048 + 1024, __builtin_bit_cast(u32x4, hi));
        }
    }
    lds_barrier();
    {
      const unsigned rcv = XIMG + idx * 12288 + lane * 16;
      f32x4 plo[3][2], phi[3][2];
#pragma unroll
      for (int t = 0; t < 3; ++t)
#pragma unroll
        for (int rr = 0; rr < 2; ++rr) {
          plo[t][rr] = __builtin_bit_cast(f32x4, lds_ld128(rcv + (t * 2 + rr) * 2048));
          phi[t][rr] = __builtin_bit_cast(f32x4, lds_ld128(rcv + (t * 2 + rr) * 2048 + 1024));
        }
#pragma unroll
      for (int t = 0; t < 3; ++t)
#pragma unroll
        for (int rr = 0; rr < 2; ++rr) {
          float v[8];
#pragma unroll
          for (int e = 0; e < 4; ++e) {
            v[e] = ((khalf ? acc[e][t][2 + rr] : acc[e][t][rr]) + plo[t][rr][e]) * SCL;
            v[4 + e] = ((khalf ? acc[4 + e][t][2 + rr] : acc[4 + e][t][rr]) + phi[t][rr][e]) * SCL;
          }
          lds_st128(rcv + (rr * 3 + t) * 1024, u32x4{pk_bf16(v[0], v[1]), pk_bf16(v[2], v[3]), pk_bf16(v[4], v[5]), pk_bf16(v[6], v[7])});
        }
    }
    STAMP(31);
    lds_barrier();
  }
#pragma unroll
  for (int s = 0; s < 4; ++s)
#pragma unroll
    for (int t = 0; t < 3; ++t) qf[s][t] = lds_ld128(XIMG + (hh * 2 + (s >> 1)) * 12288 + ((s & 1) * 3 + t) * 1024 + lane * 16);
  STAMPR(1);

  unsigned ka[4];
#pragma unroll
  for (int s = 0; s < 4; ++s) ka[s] = (32 * jq + i16) * 256 + (((4 * g + s) ^ i16) << 4);
  unsigned va[2];
  {
    const int q_ = (lane >> 2) & 3, p = lane & 3;
#pragma unroll
    for (int dt = 0; dt < 2; ++dt)
      va[dt] = (4 * g + q_) * 256 + (((4 * jq + 2 * dt + (p >> 1)) ^ (2 * ((4 * g + q_) & 7))) << 4) + 8 * (p & 1);
  }
  const unsigned pw = PB + hh * 12288 + jq * 3072 + lane * 16;
  const unsigned pr = PB + lane * 16;

  f32x4 O[2][3];
#pragma unroll
  for (int dt = 0; dt < 2; ++dt)
#pragma unroll
    for (int t = 0; t < 3; ++t) O[dt][t] = f32x4{0.f, 0.f, 0.f, 0.f};
  float lsum[3] = {0.f, 0.f, 0.f};
  f32x4 nm[3];
  f32x4 ps[3];
  const u32x4 ones = {0x3F803F80u, 0x3F803F80u, 0x3F803F80u, 0x3F803F80u};
  const f32x4 zero4 = {0.f, 0.f, 0.f, 0.f};

  auto tile_max = [&](const f32x4(&S)[2][3], int t) {
    float m = fmaxf(fmaxf(S[0][t][0], S[0][t][1]), S[0][t][2]);
    m = fmaxf(fmaxf(m, S[0][t][3]), S[1][t][0]);
    m = fmaxf(fmaxf(m, S[1][t][1]), S[1][t][2]);
    return fmaxf(m, S[1][t][3]);
  };
  auto head_max = [&](const f32x4(&S)[2][3], float (&m)[3]) {
#pragma unroll
    for (int t = 0; t < 3; ++t) {
      const float v = grp_max(tile_max(S, t));
      if (lane < 16) lds_stf(MX_OFF + ((hh * 4 + jq) * 3 + t) * 64 + lane * 4, v);
    }
    lds_barrier();
#pragma unroll
    for (int t = 0; t < 3; ++t) {
      float v = lds_ldf(MX_OFF + ((hh * 4 + 0) * 3 + t) * 64 + i16 * 4);
#pragma unroll
      for (int j = 1; j < 4; ++j) v = fmaxf(v, lds_ldf(MX_OFF + ((hh * 4 + j) * 3 + t) * 64 + i16 * 4));
      m[t] = v;
    }
  };
  auto p_unit = [&](const f32x4& s) { return u32x2{pk_bf16(ex2(s[0]), ex2(s[1])), pk_bf16(ex2(s[2]), ex2(s[3]))}; };
  auto p_put = [&](int t, u32x2 a, u32x2 c) {
    const u32x4 f = {a[0], a[1], c[0], c[1]};
    lds_st128(pw + t * 1024, f);
    return f;
  };
  auto pv_load = [&](unsigned soff, unsigned poff, int ks, u32x4 (&pf)[3], u32x4 (&vf)[2]) {
#pragma unroll
    for (int t = 0; t < 3; ++t) pf[t] = lds_ld128(pr + poff + ks * 3072 + t * 1024);
#pragma unroll
    for (int dt = 0; dt < 2; ++dt) {
      const u32x2 v0 = lds_tr(va[dt] + soff + ks * 8192);
      const u32x2 v1 = lds_tr(va[dt] + soff + ks * 8192 + 4096);
      vf[dt] = u32x4{v0[0], v0[1], v1[0], v1[1]};
    }
  };
  auto pv_mma = [&](const u32x4 (&pf)[3], const u32x4 (&vf)[2], f32x4(&A)[2][3]) {
#pragma unroll
    for (int dt = 0; dt < 2; ++dt)
#pragma unroll
      for (int t = 0; t < 3; ++t) A[dt][t] = mfma(vf[dt], pf[t], A[dt][t]);
  };

  unsigned vx[4], vy[4];
#pragma unroll
  for (int i = 0; i < 4; ++i) { vx[i] = voffV[i & 1]; vy[i] = voffK[i]; }
#define IC(v) std::integral_constant<int, (v)>{}

  f32x4 SA[2][3], SB_[2][3];
  wait_barrier<16>();
  STAMP(2);
  static_for<0, 4>([&](auto ic) { constexpr int i = decltype(ic)::value; dma_nt<i * 1024>(voffK[i], kbase + 2 * 32768, w4k + 3 * SLOT); });
#pragma unroll
  for (int kt = 0; kt < 2; ++kt)
#pragma unroll
    for (int s = 0; s < 4; ++s) {
      const u32x4 kf = lds_ld128(ka[s] + kt * 4096);
#pragma unroll
      for (int t = 0; t < 3; ++t) SA[kt][t] = mfma(kf, qf[s][t], s == 0 ? zero4 : SA[kt][t]);
    }
  {
    float m[3];
    head_max(SA, m);
#pragma unroll
    for (int t = 0; t < 3; ++t) {
      nm[t] = f32x4{-m[t], -m[t], -m[t], -m[t]};
      SA[0][t] += nm[t];
      SA[1][t] += nm[t];
    }
  }

  auto xphase = [&](auto ksc, auto dsc, auto lastc, f32x4(&SC)[2][3], f32x4(&SN)[2][3], const char* src, int t) {
    constexpr unsigned ko = decltype(ksc)::value * SLOT;
    constexpr bool LAST = decltype(lastc)::value;
    const unsigned m0v = w4k + decltype(dsc)::value * SLOT;
    u32x4 kf[8];
    auto qk = [&](auto cc) {
      constexpr int c = decltype(cc)::value, kt = c >> 2, s = c & 3;
      if constexpr (!LAST) {
#pragma unroll
        for (int tt = 0; tt < 3; ++tt) SN[kt][tt] = mfma(kf[c], qf[s][tt], s == 0 ? nm[tt] : SN[kt][tt]);
      }
    };
    if constexpr (!LAST) {
#pragma unroll
      for (int c = 0; c < 4; ++c) kf[c] = lds_ld128(ka[c] + ko);
    }
    SB();
    u32x2 a0 = p_unit(SC[0][0]), a1 = p_unit(SC[1][0]);
    const u32x4 f0 = p_put(0, a0, a1);
    SB();
    if constexpr (!LAST) {
#pragma unroll
      for (int c = 4; c < 8; ++c) kf[c] = lds_ld128(ka[c & 3] + ko + 4096);
    }
    LDMA(dma_nt<0>(vx[0], src, m0v));
    SB();
    qk(IC(0));
    a0 = p_unit(SC[0][1]);
    SB();
    qk(IC(1));
    a1 = p_unit(SC[1][1]);
    const u32x4 f1 = p_put(1, a0, a1);
    ps[0] = mfma(ones, f0, zero4);
    SB();
    LDMA(dma_nt<1024>(vx[1], src, m0v));
    SB();
    qk(IC(2));
    a0 = p_unit(SC[0][2]);
    SB();
    qk(IC(3));
    a1 = p_unit(SC[1][2]);
    const u32x4 f2 = p_put(2, a0, a1);
    ps[1] = mfma(ones, f1, zero4);
    SB();
    LDMA(dma_nt<2048>(vx[2], src, m0v));
    SB();
    qk(IC(4));
    ps[2] = mfma(ones, f2, zero4);
    SB();
    qk(IC(5));
    SB();
    LDMA(dma_nt<3072>(vx[3], src, m0v));
    SB();
    qk(IC(6));
    {
      const unsigned u0 = f2u(ps[0][0]), u1 = f2u(ps[1][0]), u2 = f2u(ps[2][0]);
      const unsigned um = max(max(u0, u1), u2);
      if (um > SUM_CAP) lds_stu(FL_OFF + t * 4, 1u);
    }
    SB();
    qk(IC(7));
  };
  auto yphase = [&](auto vsc, auto dsc, auto lastc, f32x4(&SC)[2][3], f32x4(&SN)[2][3], const char* src, int t) {
    constexpr unsigned vo = decltype(vsc)::value * SLOT;
    constexpr bool LAST = decltype(lastc)::value;
    const unsigned m0v = w4k + decltype(dsc)::value * SLOT;
    const unsigned po = hh * 12288;
    const unsigned flagv = lds_ldu(FL_OFF + t * 4);
    u32x4 pf0[3], vf0[2], pf1[3], vf1[2];
    pv_load(vo, po, 0, pf0, vf0);
    pv_load(vo, po, 1, pf1, vf1);
    SB();
    if (__builtin_amdgcn_readfirstlane(flagv)) {
      float m[3];
      head_max(SC, m);
#pragma unroll
      for (int tt = 0; tt < 3; ++tt) {
        const float d = fmaxf(m[tt], 0.f);
        const float alpha = ex2(-d);
        const f32x4 d4 = {d, d, d, d};
        lsum[tt] *= alpha;
        O[0][tt] *= alpha;
        O[1][tt] *= alpha;
        nm[tt] -= d4;
        SC[0][tt] -= d4;
        SC[1][tt] -= d4;
        if constexpr (!LAST) { SN[0][tt] -= d4; SN[1][tt] -= d4; }
        const u32x4 f = p_put(tt, p_unit(SC[0][tt]), p_unit(SC[1][tt]));
        ps[tt] = mfma(ones, f, zero4);
      }
      lds_barrier();
      pv_load(vo, po, 0, pf0, vf0);
      pv_load(vo, po, 1, pf1, vf1);
    }
    SB();
#pragma unroll
    for (int tt = 0; tt < 3; ++tt) lsum[tt] += ps[tt][0];
    pv_mma(pf0, vf0, O);
    SB();
    pv_load(vo, po, 2, pf0, vf0);
    if constexpr (!LAST) LDMA(dma_nt<0>(vy[0], src, m0v));
    SB();
    pv_mma(pf1, vf1, O);
    SB();
    pv_load(vo, po, 3, pf1, vf1);
    if constexpr (!LAST) { LDMA(dma_nt<1024>(vy[1], src, m0v)); }
    SB();
    pv_mma(pf0, vf0, O);
    SB();
    if constexpr (!LAST) { LDMA(dma_nt<2048>(vy[2], src, m0v)); LDMA(dma_nt<3072>(vy[3], src, m0v)); }
    SB();
    pv_mma(pf1, vf1, O);
  };
  auto set_w = [&](unsigned (&v)[4]) {
#pragma unroll
    for (int i = 0; i < 4; ++i) v[i] = (unsigned)((16 * w + g) * 1024 + i * 3072 + ((i16 ^ (2 * (4 * (i & 1) + g))) << 4));
  };
  auto wsrc = [&](int wi) { return wobase + (size_t)(wi >> 1) * 128 * 1024 + (wi & 1) * 256; };

#pragma unroll 1
  for (int t2 = 0; t2 < 6; t2 += 2) {
    wait_barrier<8>();
    STAMP(3 + 2 * t2);
    xphase(IC(1), IC(0), std::false_type{}, SA, SB_, vbase + (size_t)(t2 + 1) * 32768, t2);
    DSTAMP(25);
    wait_barrier<8>();
    STAMP(4 + 2 * t2);
    yphase(IC(2), IC(1), std::false_type{}, SA, SB_, kbase + (size_t)(t2 + 3) * 32768, t2);
    DSTAMP(26);
    wait_barrier<8>();
    STAMP(5 + 2 * t2);
    xphase(IC(3), IC(2), std::false_type{}, SB_, SA, vbase + (size_t)(t2 + 2) * 32768, t2 + 1);
    const char* ysrc = kbase + (size_t)(t2 + 4) * 32768;
    if (t2 == 4) {
      ysrc = vbase + (size_t)7 * 32768;
#pragma unroll
      for (int i = 0; i < 4; ++i) vy[i] = vx[i];
    }
    DSTAMP(27);
    wait_barrier<8>();
    STAMP(6 + 2 * t2);
    yphase(IC(0), IC(3), std::false_type{}, SB_, SA, ysrc, t2 + 1);
    DSTAMP(28);
  }
  set_w(vx);
#pragma unroll
  for (int i = 0; i < 4; ++i) vy[i] = vx[i];
  wait_barrier<8>();
  STAMP(15);
  xphase(IC(1), IC(0), std::false_type{}, SA, SB_, wsrc(0), 6);
  wait_barrier<8>();
  STAMP(16);
  yphase(IC(2), IC(1), std::false_type{}, SA, SB_, wsrc(1), 6);
  wait_barrier<8>();
  STAMP(17);
  xphase(IC(3), IC(2), std::true_type{}, SB_, SA, wsrc(2), 7);
  wait_barrier<12>();
  STAMP(18);
  yphase(IC(3), IC(3), std::true_type{}, SB_, SA, wsrc(3), 7);

#pragma unroll
  for (int t = 0; t < 3; ++t)
    if (lane < 16) lds_stf(LS_OFF + ((hh * 4 + jq) * 3 + t) * 64 + lane * 4, lsum[t]);
  lds_barrier();
  STAMPR(19);
  float lt[3];
#pragma unroll
  for (int t = 0; t < 3; ++t) {
    float s = 0.f;
#pragma unroll
    for (int j = 0; j < 4; ++j) s += lds_ldf(LS_OFF + ((hh * 4 + j) * 3 + t) * 64 + i16 * 4);
    lt[t] = s;
  }

  const unsigned tag = (unsigned)__builtin_amdgcn_readfirstlane(epoch_v) + 1u;
  const __amdgpu_buffer_rsrc_t ws_rs = __builtin_amdgcn_make_buffer_rsrc((void*)ws, 0, (unsigned)(WS_IN2 + (size_t)256 * 32768), 0x00020000);

  float lam[3];
  u32x4 mine[3];
#pragma unroll
  for (int t = 0; t < 3; ++t) {
    const float inv = 1.0f / lt[t];
    lam[t] = __builtin_amdgcn_logf(lt[t]) - nm[t][0];
#pragma unroll
    for (int dt = 0; dt < 2; ++dt) O[dt][t] *= inv;
    mine[t] = u32x4{pk_bf16(O[0][t][0], O[0][t][1]), pk_bf16(O[0][t][2], O[0][t][3]), pk_bf16(O[1][t][0], O[1][t][1]),
                    pk_bf16(O[1][t][2], O[1][t][3])};
  }
  {
    const float l0 = lam[0], l1 = lam[1], l2 = lam[2];
    const unsigned lw = __builtin_bit_cast(unsigned, (lane & 32) ? l2 : ((lane & 16) ? l1 : l0));
    unsigned h1, h2;
    hash13(mine[0], mine[1], mine[2], lw, tag, h1, h2);
    const unsigned dst = (unsigned)(WS_IN1 + (size_t)(bid ^ 16) * 32768 + w * 4096 + lane * 16);
    __builtin_amdgcn_raw_buffer_store_b128(mine[0], ws_rs, dst, 0, 16);
    __builtin_amdgcn_raw_buffer_store_b128(mine[1], ws_rs, dst + 1024, 0, 16);
    __builtin_amdgcn_raw_buffer_store_b128(mine[2], ws_rs, dst + 2048, 0, 16);
    __builtin_amdgcn_raw_buffer_store_b128(u32x4{lw, h1, h2, tag}, ws_rs, dst + 3072, 0, 16);
  }
  static_for<0, 4>([&](auto ic) { constexpr int i = decltype(ic)::value; dma_def<i * 1024>(vx[i], wsrc(3), w4k + 3 * SLOT); });
  u32x4 th[4];
  recv4<1024>(ws_rs, (unsigned)(WS_IN1 + (size_t)bid * 32768 + w * 4096 + lane * 16), tag, th);
  STAMPR(20);
  {
    const unsigned u = th[3][0];
    auto a = __builtin_amdgcn_permlane32_swap(u, u, false, false);
    auto c0 = __builtin_amdgcn_permlane16_swap(a[0], a[0], false, false);
    auto c1 = __builtin_amdgcn_permlane16_swap(a[1], a[1], false, false);
    const float lb[3] = {u2f(c0[0]), u2f(c0[1]), u2f(c1[0])};
#pragma unroll
    for (int t = 0; t < 3; ++t) {
      const float mx = fmaxf(lam[t], lb[t]);
      float wa = ex2(lam[t] - mx), wb = ex2(lb[t] - mx);
      const float inv = 1.0f / (wa + wb);
      wa *= inv;
      wb *= inv;
      float m[8];
#pragma unroll
      for (int k = 0; k < 4; ++k) {
        m[2 * k] = wa * O[k >> 1][t][2 * (k & 1)] + wb * bf_lo(th[t][k]);
        m[2 * k + 1] = wa * O[k >> 1][t][2 * (k & 1) + 1] + wb * bf_hi(th[t][k]);
      }
      lds_st128(pw + t * 1024, u32x4{pk_bf16(m[0], m[1]), pk_bf16(m[2], m[3]), pk_bf16(m[4], m[5]), pk_bf16(m[6], m[7])});
    }
  }
  wait_barrier<0>();
  STAMP(21);

  f32x4 A[2][3];
#pragma unroll
  for (int dt = 0; dt < 2; ++dt)
#pragma unroll
    for (int t = 0; t < 3; ++t) A[dt][t] = zero4;
  {
    u32x4 pf0[3], vf0[2], pf1[3], vf1[2];
    pv_load((0 + hh) * SLOT, 0, 0, pf0, vf0);
    pv_load((0 + hh) * SLOT, 0, 1, pf1, vf1);
#pragma unroll
    for (int st = 0; st < 8; st += 2) {
      pv_mma(pf0, vf0, A);
      if (st + 2 < 8) pv_load((2 * ((st + 2) >> 2) + hh) * SLOT, ((st + 2) >> 2) * 12288, (st + 2) & 3, pf0, vf0);
      pv_mma(pf1, vf1, A);
      if (st + 3 < 8) pv_load((2 * ((st + 3) >> 2) + hh) * SLOT, ((st + 3) >> 2) * 12288, (st + 3) & 3, pf1, vf1);
    }
  }
  lds_barrier();
  STAMP(22);

  {
    u32x4 f[3];
#pragma unroll
    for (int t = 0; t < 3; ++t)
      f[t] = u32x4{pk_bf16(A[0][t][0], A[0][t][1]), pk_bf16(A[0][t][2], A[0][t][3]), pk_bf16(A[1][t][0], A[1][t][1]),
                   pk_bf16(A[1][t][2], A[1][t][3])};
    unsigned h1, h2;
    hash13(f[0], f[1], f[2], 0u, tag, h1, h2);
    const int rho = 2 * w + (i16 >> 3);
    const int rbid = (rho >> 1) + 8 * ((rho & 1) + 2 * kh + 4 * b);
    const int pidx = kvh * 2 + hp;
    const unsigned dst = (unsigned)(WS_IN2 + (size_t)rbid * 32768 + pidx * 2048 + (8 * g + (i16 & 7)) * 16);
    __builtin_amdgcn_raw_buffer_store_b128(f[0], ws_rs, dst, 0, 16);
    __builtin_amdgcn_raw_buffer_store_b128(f[1], ws_rs, dst + 512, 0, 16);
    __builtin_amdgcn_raw_buffer_store_b128(f[2], ws_rs, dst + 1024, 0, 16);
    __builtin_amdgcn_raw_buffer_store_b128(u32x4{0u, h1, h2, tag}, ws_rs, dst + 1536, 0, 16);
  }
  {
    recv4<512>(ws_rs, (unsigned)(WS_IN2 + (size_t)bid * 32768 + (tid >> 5) * 2048 + (tid & 31) * 16), tag, th);
    STAMPR(23);
#pragma unroll
    for (int t = 0; t < 3; ++t) {
      f32x4 r;
#pragma unroll
      for (int k = 0; k < 2; ++k) {
        const float a0 = half_sum(bf_lo(th[t][k])), a1 = half_sum(bf_hi(th[t][k]));
        const float b0 = half_sum(bf_lo(th[t][2 + k])), b1 = half_sum(bf_hi(th[t][2 + k]));
        r[2 * k] = lane < 32 ? a0 : b0;
        r[2 * k + 1] = lane < 32 ? a1 : b1;
      }
      lds_st128(PB + w * 3072 + t * 1024 + lane * 16, __builtin_bit_cast(u32x4, r));
    }
  }
  lds_barrier();
  if (tid < 192) {
    const int t = tid >> 6;
    f32x4 s = __builtin_bit_cast(f32x4, lds_ld128(PB + t * 1024 + lane * 16));
#pragma unroll
    for (int v = 1; v < 8; ++v) s += __builtin_bit_cast(f32x4, lds_ld128(PB + v * 3072 + t * 1024 + lane * 16));
    const int rho = kvh * 2 + hp;
    const int nt = lane >> 5, l32 = lane & 31, gg = l32 >> 3, q = 8 * (rho & 1) + (l32 & 7);
    const int col = 256 * kh + 32 * (rho >> 1) + 16 * nt + 4 * gg;
    u32x2 o = {pk_bf16(s[0], s[1]), pk_bf16(s[2], s[3])};
    *(u32x2*)(out + ((size_t)b * SQ + 16 * t + q) * DM + col) = o;
  }
  STAMPR(24);
  STAMP_DUMP();
  if (tid == 0 && kvh == 0 && hp == 0 && kh == 0) __hip_atomic_store(epoch_ptr, tag, __ATOMIC_RELAXED, __HIP_MEMORY_SCOPE_AGENT);
}

}

extern "C" void kernel_launch(void* const* d_in, const int* in_sizes, int n_in, void* d_out, int out_size, void* d_ws,
                              size_t ws_size, hipStream_t stream) {
  (void)in_sizes; (void)n_in; (void)out_size; (void)ws_size;
  (void)hipFuncSetAttribute((const void*)gqa_fused_kernel, hipFuncAttributeMaxDynamicSharedMemorySize, LDS_BYTES);
  gqa_fused_kernel<<<256, 512, LDS_BYTES, stream>>>((const uint16_t*)d_in[0], (const uint16_t*)d_in[1], (const uint16_t*)d_in[2],
                                                    (const uint16_t*)d_in[3], (const uint16_t*)d_in[4], (uint16_t*)d_out,
                                                    (unsigned char*)d_ws);
}
```

```cpp
#include <hip/hip_runtime.h>
#include <hip/hip_bf16.h>
#include <stdint.h>
#include <type_traits>

#define AS3 __attribute__((address_space(3)))
typedef __attribute__((ext_vector_type(8))) __bf16 bf16x8;
typedef __attribute__((ext_vector_type(2))) __bf16 bf16x2;
typedef __attribute__((ext_vector_type(4))) float f32x4;
typedef __attribute__((ext_vector_type(2))) float f32x2;
typedef __attribute__((ext_vector_type(4))) unsigned u32x4;
typedef __attribute__((ext_vector_type(2))) unsigned u32x2;
typedef __attribute__((ext_vector_type(4))) short i16x4;

#define STAMP(i) do {} while (0)
#define STAMP_DUMP() do {} while (0)

namespace {

constexpr int SQ = 48, DM = 512, SK = 2048, DH = 128, NQ = 4096;

constexpr unsigned SLOT = 32768;
constexpr unsigned PB = 131072;
constexpr unsigned MISC = 155648;
constexpr unsigned MX_OFF = MISC;
constexpr unsigned LS_OFF = MISC + 1536;
constexpr unsigned FL_OFF = MISC + 3072;
constexpr unsigned XIMG = 98304;
constexpr unsigned QIMG = 98304;
constexpr unsigned LDS_BYTES = 163840;

constexpr size_t WS_EPOCH = 0;
constexpr size_t WS_IN1 = 4096;
constexpr size_t WS_IN2 = WS_IN1 + (size_t)256 * 32768;

constexpr float SCL = 0.08838834764831845f * 1.4426950408889634f;
constexpr float THR = 16.0f;
constexpr unsigned SPIN_CAP = 1u << 16;

__device__ __forceinline__ u32x4 lds_ld128(unsigned a) { return *(const AS3 u32x4*)(size_t)a; }
__device__ __forceinline__ void lds_st128(unsigned a, u32x4 v) { *(AS3 u32x4*)(size_t)a = v; }
__device__ __forceinline__ float lds_ldf(unsigned a) { return *(const AS3 float*)(size_t)a; }
__device__ __forceinline__ void lds_stf(unsigned a, float v) { *(AS3 float*)(size_t)a = v; }
__device__ __forceinline__ unsigned lds_ldu(unsigned a) { return *(const AS3 unsigned*)(size_t)a; }
__device__ __forceinline__ void lds_stu(unsigned a, unsigned v) { *(AS3 unsigned*)(size_t)a = v; }
__device__ __forceinline__ u32x2 lds_tr(unsigned a) {
  i16x4 r = __builtin_amdgcn_ds_read_tr16_b64_v4i16((AS3 i16x4*)(size_t)a);
  return __builtin_bit_cast(u32x2, r);
}
__device__ __forceinline__ f32x4 mfma(u32x4 a, u32x4 b, f32x4 c) {
  return __builtin_amdgcn_mfma_f32_16x16x32_bf16(__builtin_bit_cast(bf16x8, a), __builtin_bit_cast(bf16x8, b), c, 0, 0, 0);
}
__device__ __forceinline__ unsigned pk_bf16(float lo, float hi) {
  f32x2 v = {lo, hi};
  bf16x2 r = __builtin_convertvector(v, bf16x2);
  return __builtin_bit_cast(unsigned, r);
}
__device__ __forceinline__ float u2f(unsigned u) { return __builtin_bit_cast(float, u); }
__device__ __forceinline__ unsigned f2u(float f) { return __builtin_bit_cast(unsigned, f); }
__device__ __forceinline__ float bf_lo(unsigned u) { return __builtin_bit_cast(float, u << 16); }
__device__ __forceinline__ float bf_hi(unsigned u) { return __builtin_bit_cast(float, u & 0xffff0000u); }

template <unsigned LDS_IMM>
__device__ __forceinline__ void dma_nt(unsigned voff, const void* sbase, unsigned lds_s) {
  unsigned keep;
  asm volatile("s_mov_b32 %0, m0\n\ts_add_u32 m0, %3, %4\n\ts_nop 0\n\tglobal_load_lds_dwordx4 %1, %2 nt\n\ts_mov_b32 m0, %0"
               : "=&s"(keep) : "v"(voff), "s"(sbase), "s"(lds_s), "n"(LDS_IMM) : "memory", "scc");
}
template <unsigned LDS_IMM>
__device__ __forceinline__ void dma_def(unsigned voff, const void* sbase, unsigned lds_s) {
  unsigned keep;
  asm volatile("s_mov_b32 %0, m0\n\ts_add_u32 m0, %3, %4\n\ts_nop 0\n\tglobal_load_lds_dwordx4 %1, %2\n\ts_mov_b32 m0, %0"
               : "=&s"(keep) : "v"(voff), "s"(sbase), "s"(lds_s), "n"(LDS_IMM) : "memory", "scc");
}
template <int I, int N, class F>
__device__ __forceinline__ void static_for(F&& f) {
  if constexpr (I < N) {
    f(std::integral_constant<int, I>{});
    static_for<I + 1, N>(f);
  }
}
template <int N>
__device__ __forceinline__ void wait_barrier() {
  asm volatile("s_waitcnt vmcnt(%0) lgkmcnt(0)\n\ts_barrier" ::"n"(N) : "memory");
}
__device__ __forceinline__ void lds_barrier() { asm volatile("s_waitcnt lgkmcnt(0)\n\ts_barrier" ::: "memory"); }

__device__ __forceinline__ float grp_max(float v) {
  unsigned u = __builtin_bit_cast(unsigned, v);
  auto a = __builtin_amdgcn_permlane16_swap(u, u, false, false);
  v = fmaxf(u2f(a[0]), u2f(a[1]));
  u = __builtin_bit_cast(unsigned, v);
  auto c = __builtin_amdgcn_permlane32_swap(u, u, false, false);
  return fmaxf(u2f(c[0]), u2f(c[1]));
}
__device__ __forceinline__ float grp_sum(float v) {
  unsigned u = __builtin_bit_cast(unsigned, v);
  auto a = __builtin_amdgcn_permlane16_swap(u, u, false, false);
  v = u2f(a[0]) + u2f(a[1]);
  u = __builtin_bit_cast(unsigned, v);
  auto c = __builtin_amdgcn_permlane32_swap(u, u, false, false);
  return u2f(c[0]) + u2f(c[1]);
}
__device__ __forceinline__ float half_sum(float v) {
  unsigned u = __builtin_bit_cast(unsigned, v);
  auto c = __builtin_amdgcn_permlane32_swap(u, u, false, false);
  return u2f(c[0]) + u2f(c[1]);
}

__device__ __forceinline__ unsigned rotl5(unsigned h) { return __builtin_amdgcn_alignbit(h, h, 27); }
__device__ __forceinline__ void hash13(const u32x4& a, const u32x4& b, const u32x4& c, unsigned d, unsigned tag,
                                       unsigned& h1, unsigned& h2) {
  unsigned x = tag ^ 0x9E3779B9u, y = ~tag;
#pragma unroll
  for (int k = 0; k < 4; ++k) { x = (rotl5(x) ^ a[k]) + 0x85EBCA6Bu; }
#pragma unroll
  for (int k = 0; k < 2; ++k) { x = (rotl5(x) ^ b[k]) + 0x85EBCA6Bu; }
#pragma unroll
  for (int k = 2; k < 4; ++k) { y = (rotl5(y) ^ b[k]) + 0xC2B2AE35u; }
#pragma unroll
  for (int k = 0; k < 4; ++k) { y = (rotl5(y) ^ c[k]) + 0xC2B2AE35u; }
  y = (rotl5(y) ^ d) + 0xC2B2AE35u;
  h1 = (x ^ rotl5(y)) + y;
  h2 = (a[0] + a[1] + a[2]) + (a[3] + b[0] + b[1]) + (b[2] + b[3] + c[0]) + (c[1] + c[2] + c[3]) + (d + 2u * tag + 1u);
}

__global__ __launch_bounds__(512, 2) void gqa_fused_kernel(const uint16_t* __restrict__ x, const uint16_t* __restrict__ kc,
                                                           const uint16_t* __restrict__ vc, const uint16_t* __restrict__ wq,
                                                           const uint16_t* __restrict__ wo, uint16_t* __restrict__ out,
                                                           unsigned char* __restrict__ ws) {
  constexpr unsigned L0 = 0;
  const int tid = threadIdx.x;
  const int lane = tid & 63;
  const int w = __builtin_amdgcn_readfirstlane(tid >> 6);
  const int bid = blockIdx.x;
  const int kvh = bid & 7, slot = bid >> 3, hp = slot & 1, kh = (slot >> 1) & 1, b = slot >> 2;
  const int g = lane >> 4, i16 = lane & 15;
  const int hh = w >> 2, jq = w & 3;
  const int head0 = kvh * 4 + hp * 2;

  STAMP(0);
  unsigned* epoch_ptr = (unsigned*)(ws + WS_EPOCH + (size_t)b * 256);
  const unsigned epoch_v = __hip_atomic_load(epoch_ptr, __ATOMIC_RELAXED, __HIP_MEMORY_SCOPE_AGENT);

  const char* kbase = (const char*)kc + ((size_t)(b * 8 + kvh) * SK + kh * 1024) * (DH * 2);
  const char* vbase = (const char*)vc + ((size_t)(b * 8 + kvh) * SK + kh * 1024) * (DH * 2);
  const char* wobase = (const char*)wo + ((size_t)head0 * DH * DM + kh * 256) * 2;

  const unsigned voffK = (unsigned)(w * 1024 + g * 256 + ((i16 ^ ((4 * (w & 3) + g) & 15)) << 4));
  const unsigned swzV = (unsigned)((i16 ^ (2 * ((4 * (w & 1) + g) & 7))) << 4);
  const unsigned voffV = (unsigned)(w * 1024 + g * 256) + swzV;
  const unsigned voffW = (unsigned)((4 * w + g) * 1024) + swzV;

  const unsigned w1k = (unsigned)(w * 1024);
  u32x4 qf[4][3];

  if (w < 4) {
    const char* xb = (const char*)x + (size_t)b * SQ * DM * 2;
    static_for<0, 12>([&](auto rc) {
      constexpr int r = decltype(rc)::value;
      const int row = w * 12 + r;
      dma_def<XIMG + r * 1024>((unsigned)((lane ^ (row & 15)) << 4), xb + row * 1024, (unsigned)(w * 12288));
    });
    static_for<0, 8>([&](auto ic) { constexpr int i = decltype(ic)::value; dma_nt<0 * SLOT + i * 4096>(voffK, kbase + i * 4096, w1k); });
    static_for<0, 8>([&](auto ic) { constexpr int i = decltype(ic)::value; dma_nt<1 * SLOT + i * 4096>(voffK, kbase + 32768 + i * 4096, w1k); });
    static_for<0, 8>([&](auto ic) { constexpr int i = decltype(ic)::value; dma_nt<2 * SLOT + i * 4096>(voffV, vbase + i * 4096, w1k); });
    if (lane < 16) lds_stu(L0 + FL_OFF + lane * 4, 0u);
    wait_barrier<24>();
    lds_barrier();
    lds_barrier();
  } else {
    const int idx = w - 4, qh = idx >> 1, half = idx & 1;
    const __amdgpu_buffer_rsrc_t wq_rs = __builtin_amdgcn_make_buffer_rsrc((void*)wq, 0, DM * NQ * 2, 0x00020000);
    const unsigned wq_voff = (unsigned)((8 * g) * (NQ * 2) + ((head0 + qh) * DH + 64 * half + 4 * i16) * 2);
    constexpr int PRE = 6;
    u32x2 wr[16][8];
#pragma unroll
    for (int ks = 0; ks < PRE; ++ks)
#pragma unroll
      for (int j = 0; j < 8; ++j)
        wr[ks][j] = __builtin_bit_cast(u32x2, __builtin_amdgcn_raw_buffer_load_b64(wq_rs, wq_voff, (32 * ks + j) * (NQ * 2), 0));
    wait_barrier<63>();
    f32x4 acc[4][3];
#pragma unroll
    for (int e = 0; e < 4; ++e)
#pragma unroll
      for (int t = 0; t < 3; ++t) acc[e][t] = f32x4{0.f, 0.f, 0.f, 0.f};
    unsigned xa[4];
#pragma unroll
    for (int k = 0; k < 4; ++k) xa[k] = L0 + XIMG + i16 * 1024 + (((4 * k + g) ^ i16) << 4);
#pragma unroll
    for (int ks = 0; ks < 16; ++ks) {
      if (ks + PRE < 16) {
#pragma unroll
        for (int j = 0; j < 8; ++j)
          wr[ks + PRE][j] = __builtin_bit_cast(u32x2, __builtin_amdgcn_raw_buffer_load_b64(wq_rs, wq_voff, (32 * (ks + PRE) + j) * (NQ * 2), 0));
      }
      u32x4 xf[3];
#pragma unroll
      for (int t = 0; t < 3; ++t) xf[t] = lds_ld128(xa[ks & 3] + (ks >> 2) * 256 + t * 16384);
#pragma unroll
      for (int e = 0; e < 4; ++e) {
        u32x4 af;
#pragma unroll
        for (int r = 0; r < 4; ++r)
          af[r] = __builtin_amdgcn_perm(wr[ks][2 * r + 1][e >> 1], wr[ks][2 * r][e >> 1], (e & 1) ? 0x07060302u : 0x05040100u);
#pragma unroll
        for (int t = 0; t < 3; ++t) acc[e][t] = mfma(af, xf[t], acc[e][t]);
      }
      __builtin_amdgcn_sched_barrier(0);
    }
    lds_barrier();
#pragma unroll
    for (int u = 0; u < 2; ++u)
#pragma unroll
      for (int t = 0; t < 3; ++t) {
        u32x4 f;
        f[0] = pk_bf16(acc[0][t][2 * u], acc[1][t][2 * u]);
        f[1] = pk_bf16(acc[2][t][2 * u], acc[3][t][2 * u]);
        f[2] = pk_bf16(acc[0][t][2 * u + 1], acc[1][t][2 * u + 1]);
        f[3] = pk_bf16(acc[2][t][2 * u + 1], acc[3][t][2 * u + 1]);
        lds_st128(L0 + QIMG + qh * 12288 + (2 * half + u) * 3072 + t * 1024 + lane * 16, f);
      }
    lds_barrier();
  }
  STAMP(1);
#pragma unroll
  for (int s = 0; s < 4; ++s)
#pragma unroll
    for (int t = 0; t < 3; ++t) qf[s][t] = lds_ld128(L0 + QIMG + hh * 12288 + s * 3072 + t * 1024 + lane * 16);

  unsigned ka[4];
#pragma unroll
  for (int s = 0; s < 4; ++s) ka[s] = L0 + (32 * jq + i16) * 256 + (((8 * (s >> 1) + 2 * g + (s & 1)) ^ i16) << 4);
  unsigned va[2];
  {
    const int q_ = (lane >> 2) & 3, p = lane & 3;
#pragma unroll
    for (int dt = 0; dt < 2; ++dt)
      va[dt] = L0 + (4 * g + q_) * 256 + (((4 * jq + 2 * dt + (p >> 1)) ^ (2 * ((4 * g + q_) & 7))) << 4) + 8 * (p & 1);
  }
  const unsigned pw = L0 + PB + hh * 12288 + jq * 3072 + lane * 16;
  const unsigned pr = L0 + PB + lane * 16;

  f32x4 O[2][3];
#pragma unroll
  for (int dt = 0; dt < 2; ++dt)
#pragma unroll
    for (int t = 0; t < 3; ++t) O[dt][t] = f32x4{0.f, 0.f, 0.f, 0.f};
  float lsum[3] = {0.f, 0.f, 0.f};
  float mref[3] = {-INFINITY, -INFINITY, -INFINITY};
  float thr_raw[3] = {-INFINITY, -INFINITY, -INFINITY};

  auto qk = [&](unsigned soff, f32x4(&S)[2][3]) {
#pragma unroll
    for (int kt = 0; kt < 2; ++kt) {
#pragma unroll
      for (int s = 0; s < 4; ++s) {
        const u32x4 kf = lds_ld128(ka[s] + soff + kt * 4096);
#pragma unroll
        for (int t = 0; t < 3; ++t) S[kt][t] = mfma(kf, qf[s][t], s == 0 ? f32x4{0.f, 0.f, 0.f, 0.f} : S[kt][t]);
      }
    }
  };
  auto pv = [&](unsigned soff, unsigned poff, f32x4(&A)[2][3]) {
#pragma unroll
    for (int ks = 0; ks < 4; ++ks) {
      u32x4 pf[3];
#pragma unroll
      for (int t = 0; t < 3; ++t) pf[t] = lds_ld128(pr + poff + ks * 3072 + t * 1024);
#pragma unroll
      for (int dt = 0; dt < 2; ++dt) {
        const u32x2 v0 = lds_tr(va[dt] + soff + ks * 8192);
        const u32x2 v1 = lds_tr(va[dt] + soff + ks * 8192 + 4096);
        const u32x4 vf = {v0[0], v0[1], v1[0], v1[1]};
#pragma unroll
        for (int t = 0; t < 3; ++t) A[dt][t] = mfma(vf, pf[t], A[dt][t]);
      }
    }
  };
  auto tile_max = [&](const f32x4(&S)[2][3], int t) {
    float m = fmaxf(fmaxf(S[0][t][0], S[0][t][1]), S[0][t][2]);
    m = fmaxf(fmaxf(m, S[0][t][3]), S[1][t][0]);
    m = fmaxf(fmaxf(m, S[1][t][1]), S[1][t][2]);
    return fmaxf(m, S[1][t][3]);
  };
  auto remax = [&](const f32x4(&S)[2][3]) {
#pragma unroll
    for (int t = 0; t < 3; ++t) {
      const float m = grp_max(tile_max(S, t));
      if (lane < 16) lds_stf(L0 + MX_OFF + ((hh * 4 + jq) * 3 + t) * 64 + lane * 4, m);
    }
    lds_barrier();
#pragma unroll
    for (int t = 0; t < 3; ++t) {
      float m = lds_ldf(L0 + MX_OFF + ((hh * 4 + 0) * 3 + t) * 64 + i16 * 4);
#pragma unroll
      for (int j = 1; j < 4; ++j) m = fmaxf(m, lds_ldf(L0 + MX_OFF + ((hh * 4 + j) * 3 + t) * 64 + i16 * 4));
      const float mn = fmaxf(m * SCL, mref[t]);
      const float alpha = __builtin_amdgcn_exp2f(mref[t] - mn);
      lsum[t] *= alpha;
#pragma unroll
      for (int dt = 0; dt < 2; ++dt) O[dt][t] *= alpha;
      mref[t] = mn;
      thr_raw[t] = (mn + THR) * (1.0f / SCL);
    }
  };
  auto softmax_store = [&](const f32x4(&S)[2][3]) {
#pragma unroll
    for (int t = 0; t < 3; ++t) {
      float p[2][4];
#pragma unroll
      for (int kt = 0; kt < 2; ++kt)
#pragma unroll
        for (int r = 0; r < 4; ++r) {
          p[kt][r] = __builtin_amdgcn_exp2f(__builtin_fmaf(S[kt][t][r], SCL, -mref[t]));
          lsum[t] += p[kt][r];
        }
      u32x4 f = {pk_bf16(p[0][0], p[0][1]), pk_bf16(p[0][2], p[0][3]), pk_bf16(p[1][0], p[1][1]), pk_bf16(p[1][2], p[1][3])};
      lds_st128(pw + t * 1024, f);
    }
  };
  auto check = [&](const f32x4(&S)[2][3], int tn) {
    bool over = false;
#pragma unroll
    for (int t = 0; t < 3; ++t) over |= tile_max(S, t) > thr_raw[t];
    if (__builtin_amdgcn_ballot_w64(over) != 0ull) {
      if (lane == 0) lds_stu(L0 + FL_OFF + tn * 4, 1u);
    }
  };
  unsigned w1m = w1k;
  asm volatile("" : "+s"(w1m));
  auto req_kv = [&](const char* src, unsigned voff, auto slc) {
    constexpr unsigned sl = decltype(slc)::value;
    static_for<0, 4>([&](auto ic) { constexpr int i = decltype(ic)::value; dma_nt<sl * SLOT + i * 8192>(voff, src + i * 8192, w1m); });
  };
  auto req_w = [&](auto wic) {
    constexpr int wi = decltype(wic)::value;
    const char* src = wobase + (size_t)(wi >> 1) * 128 * 1024 + (wi & 1) * 256;
    static_for<0, 4>([&](auto ic) { constexpr int i = decltype(ic)::value; dma_def<wi * SLOT + i * 8192>(voffW, src + i * 32768, w1m); });
  };
#define IC(v) std::integral_constant<int, (v)>{}

  f32x4 SA[2][3], SB[2][3];
  wait_barrier<16>();
  STAMP(2);
  req_kv(kbase + 2 * 32768, voffK, IC(3));
  qk(0 * SLOT, SA);
  unsigned flag = 1;

#define SUPER(PAR, t, SC, SN)                                                                       \
  {                                                                                                 \
                                   \
    wait_barrier<8>();                                                                              \
    STAMP(3 + 2 * (t));                                                                             \
    if ((t) <= 5) req_kv(vbase + (size_t)((t) + 1) * 32768, voffV, IC((PAR) ? 2 : 0));                  \
    else req_w(IC((PAR) ? 2 : 0));                                                                   \
    if ((t) == 2) STAMP(27);                                                                        \
    if (flag) remax(SC);                                                                            \
    if ((t) < 7) qk(((PAR) ? 3 : 1) * SLOT, SN);                                                    \
    softmax_store(SC);                                                                              \
    if ((t) < 7) check(SN, (t) + 1);                                                                \
    else {                                                                                          \
      _Pragma("unroll") for (int tt = 0; tt < 3; ++tt) {                                            \
        const float s_ = grp_sum(lsum[tt]);                                                         \
        if (lane < 16) lds_stf(L0 + LS_OFF + ((hh * 4 + jq) * 3 + tt) * 64 + lane * 4, s_);         \
      }                                                                                             \
    }                                                                                               \
    if ((t) == 2) STAMP(28);                                                                        \
                 \
    if ((t) < 7) wait_barrier<8>(); else wait_barrier<12>();                                        \
    STAMP(4 + 2 * (t));                                                                             \
    if ((t) <= 4) req_kv(kbase + (size_t)((t) + 3) * 32768, voffK, IC((PAR) ? 3 : 1));                  \
    else if ((t) == 5) req_kv(vbase + (size_t)7 * 32768, voffV, IC(3));                                 \
    else if ((t) == 6) req_w(IC(1));                                                                    \
    if ((t) == 2) STAMP(29);                                                                        \
    flag = __builtin_amdgcn_readfirstlane(lds_ldu(L0 + FL_OFF + (((t) + 1) & 15) * 4));             \
    pv(((t) == 7 ? 3 : ((PAR) ? 0 : 2)) * SLOT, hh * 12288, O);                                     \
    if ((t) == 2) STAMP(30);                                                                        \
  }

#pragma unroll 1
  for (int t2 = 0; t2 < 8; t2 += 2) {
    SUPER(0, t2, SA, SB)
    SUPER(1, t2 + 1, SB, SA)
  }
#undef SUPER

  float lt[3];
#pragma unroll
  for (int t = 0; t < 3; ++t) {
    float s = 0.f;
#pragma unroll
    for (int j = 0; j < 4; ++j) s += lds_ldf(L0 + LS_OFF + ((hh * 4 + j) * 3 + t) * 64 + i16 * 4);
    lt[t] = s;
  }
  lds_barrier();
  STAMP(19);
  req_w(IC(3));

  const unsigned tag = (unsigned)__builtin_amdgcn_readfirstlane(epoch_v) + 1u;
  const __amdgpu_buffer_rsrc_t ws_rs = __builtin_amdgcn_make_buffer_rsrc((void*)ws, 0, (unsigned)(WS_IN2 + (size_t)256 * 32768), 0x00020000);

  float lam[3];
  u32x4 mine[3];
#pragma unroll
  for (int t = 0; t < 3; ++t) {
    const float inv = 1.0f / lt[t];
    lam[t] = mref[t] + __builtin_amdgcn_logf(lt[t]);
#pragma unroll
    for (int dt = 0; dt < 2; ++dt) O[dt][t] *= inv;
    mine[t] = u32x4{pk_bf16(O[0][t][0], O[0][t][1]), pk_bf16(O[0][t][2], O[0][t][3]), pk_bf16(O[1][t][0], O[1][t][1]),
                    pk_bf16(O[1][t][2], O[1][t][3])};
  }
  {
    const float l0 = lam[0], l1 = lam[1], l2 = lam[2];
    const unsigned lw = __builtin_bit_cast(unsigned, (lane & 32) ? l2 : ((lane & 16) ? l1 : l0));
    unsigned h1, h2;
    hash13(mine[0], mine[1], mine[2], lw, tag, h1, h2);
    const unsigned dst = (unsigned)(WS_IN1 + (size_t)(bid ^ 16) * 32768 + w * 4096 + lane * 16);
    __builtin_amdgcn_raw_buffer_store_b128(mine[0], ws_rs, dst, 0, 16);
    __builtin_amdgcn_raw_buffer_store_b128(mine[1], ws_rs, dst + 1024, 0, 16);
    __builtin_amdgcn_raw_buffer_store_b128(mine[2], ws_rs, dst + 2048, 0, 16);
    __builtin_amdgcn_raw_buffer_store_b128(u32x4{lw, h1, h2, tag}, ws_rs, dst + 3072, 0, 16);
  }
  u32x4 th[4];
  {
    const unsigned src = (unsigned)(WS_IN1 + (size_t)bid * 32768 + w * 4096 + lane * 16);
    for (unsigned spin = 0; spin < SPIN_CAP; ++spin) {
      asm volatile("" ::: "memory");
#pragma unroll
      for (int k = 0; k < 4; ++k) th[k] = __builtin_amdgcn_raw_buffer_load_b128(ws_rs, src + k * 1024, 0, 16);
      unsigned h1, h2;
      hash13(th[0], th[1], th[2], th[3][0], tag, h1, h2);
      const bool ok = (h1 == th[3][1]) & (h2 == th[3][2]);
      if (__builtin_amdgcn_ballot_w64(ok) == ~0ull) break;
      __builtin_amdgcn_s_sleep(1);
    }
  }
  STAMP(20);
  {
    const unsigned u = th[3][0];
    auto a = __builtin_amdgcn_permlane32_swap(u, u, false, false);
    auto c0 = __builtin_amdgcn_permlane16_swap(a[0], a[0], false, false);
    auto c1 = __builtin_amdgcn_permlane16_swap(a[1], a[1], false, false);
    const float lb[3] = {u2f(c0[0]), u2f(c0[1]), u2f(c1[0])};
#pragma unroll
    for (int t = 0; t < 3; ++t) {
      const float mx = fmaxf(lam[t], lb[t]);
      float wa = __builtin_amdgcn_exp2f(lam[t] - mx), wb = __builtin_amdgcn_exp2f(lb[t] - mx);
      const float inv = 1.0f / (wa + wb);
      wa *= inv;
      wb *= inv;
      float m[8];
#pragma unroll
      for (int k = 0; k < 4; ++k) {
        m[2 * k] = wa * O[k >> 1][t][2 * (k & 1)] + wb * bf_lo(th[t][k]);
        m[2 * k + 1] = wa * O[k >> 1][t][2 * (k & 1) + 1] + wb * bf_hi(th[t][k]);
      }
      lds_st128(pw + t * 1024, u32x4{pk_bf16(m[0], m[1]), pk_bf16(m[2], m[3]), pk_bf16(m[4], m[5]), pk_bf16(m[6], m[7])});
    }
  }
  wait_barrier<0>();
  STAMP(21);

  f32x4 A[2][3];
#pragma unroll
  for (int dt = 0; dt < 2; ++dt)
#pragma unroll
    for (int t = 0; t < 3; ++t) A[dt][t] = f32x4{0.f, 0.f, 0.f, 0.f};
  pv((0 + hh) * SLOT, 0, A);
  pv((2 + hh) * SLOT, 12288, A);
  lds_barrier();
  STAMP(22);

  {
    u32x4 f[3];
#pragma unroll
    for (int t = 0; t < 3; ++t)
      f[t] = u32x4{pk_bf16(A[0][t][0], A[0][t][1]), pk_bf16(A[0][t][2], A[0][t][3]), pk_bf16(A[1][t][0], A[1][t][1]),
                   pk_bf16(A[1][t][2], A[1][t][3])};
    unsigned h1, h2;
    hash13(f[0], f[1], f[2], 0u, tag, h1, h2);
    const int rho = 2 * w + (i16 >> 3);
    const int rbid = (rho >> 1) + 8 * ((rho & 1) + 2 * kh + 4 * b);
    const int pidx = kvh * 2 + hp;
    const unsigned dst = (unsigned)(WS_IN2 + (size_t)rbid * 32768 + pidx * 2048 + (8 * g + (i16 & 7)) * 16);
    __builtin_amdgcn_raw_buffer_store_b128(f[0], ws_rs, dst, 0, 16);
    __builtin_amdgcn_raw_buffer_store_b128(f[1], ws_rs, dst + 512, 0, 16);
    __builtin_amdgcn_raw_buffer_store_b128(f[2], ws_rs, dst + 1024, 0, 16);
    __builtin_amdgcn_raw_buffer_store_b128(u32x4{0u, h1, h2, tag}, ws_rs, dst + 1536, 0, 16);
  }
  {
    const unsigned src = (unsigned)(WS_IN2 + (size_t)bid * 32768 + (tid >> 5) * 2048 + (tid & 31) * 16);
    for (unsigned spin = 0; spin < SPIN_CAP; ++spin) {
      asm volatile("" ::: "memory");
#pragma unroll
      for (int k = 0; k < 4; ++k) th[k] = __builtin_amdgcn_raw_buffer_load_b128(ws_rs, src + k * 512, 0, 16);
      unsigned h1, h2;
      hash13(th[0], th[1], th[2], th[3][0], tag, h1, h2);
      const bool ok = (h1 == th[3][1]) & (h2 == th[3][2]);
      if (__builtin_amdgcn_ballot_w64(ok) == ~0ull) break;
      __builtin_amdgcn_s_sleep(1);
    }
    STAMP(23);
#pragma unroll
    for (int t = 0; t < 3; ++t) {
      f32x4 r;
#pragma unroll
      for (int k = 0; k < 2; ++k) {
        const float a0 = half_sum(bf_lo(th[t][k])), a1 = half_sum(bf_hi(th[t][k]));
        const float b0 = half_sum(bf_lo(th[t][2 + k])), b1 = half_sum(bf_hi(th[t][2 + k]));
        r[2 * k] = lane < 32 ? a0 : b0;
        r[2 * k + 1] = lane < 32 ? a1 : b1;
      }
      lds_st128(L0 + PB + w * 3072 + t * 1024 + lane * 16, __builtin_bit_cast(u32x4, r));
    }
  }
  lds_barrier();
  if (tid < 192) {
    const int t = tid >> 6;
    f32x4 s = __builtin_bit_cast(f32x4, lds_ld128(L0 + PB + t * 1024 + lane * 16));
#pragma unroll
    for (int v = 1; v < 8; ++v) s += __builtin_bit_cast(f32x4, lds_ld128(L0 + PB + v * 3072 + t * 1024 + lane * 16));
    const int rho = kvh * 2 + hp;
    const int nt = lane >> 5, l32 = lane & 31, gg = l32 >> 3, q = 8 * (rho & 1) + (l32 & 7);
    const int col = 256 * kh + 32 * (rho >> 1) + 16 * nt + 4 * gg;
    u32x2 o = {pk_bf16(s[0], s[1]), pk_bf16(s[2], s[3])};
    *(u32x2*)(out + ((size_t)b * SQ + 16 * t + q) * DM + col) = o;
  }
  STAMP(24);
  STAMP_DUMP();
  if (tid == 0 && kvh == 0 && hp == 0 && kh == 0) __hip_atomic_store(epoch_ptr, tag, __ATOMIC_RELAXED, __HIP_MEMORY_SCOPE_AGENT);
}

}

extern "C" void kernel_launch(void* const* d_in, const int* in_sizes, int n_in, void* d_out, int out_size, void* d_ws,
                              size_t ws_size, hipStream_t stream) {
  (void)in_sizes; (void)n_in; (void)out_size; (void)ws_size;
  (void)hipFuncSetAttribute((const void*)gqa_fused_kernel, hipFuncAttributeMaxDynamicSharedMemorySize, LDS_BYTES);
  gqa_fused_kernel<<<256, 512, LDS_BYTES, stream>>>((const uint16_t*)d_in[0], (const uint16_t*)d_in[1], (const uint16_t*)d_in[2],
                                                    (const uint16_t*)d_in[3], (const uint16_t*)d_in[4], (uint16_t*)d_out,
                                                    (unsigned char*)d_ws);
}
```

```cpp
#include <hip/hip_runtime.h>
#include <hip/hip_bf16.h>
#include <stdint.h>
#include <type_traits>

#define AS3 __attribute__((address_space(3)))
typedef __attribute__((ext_vector_type(8))) __bf16 bf16x8;
typedef __attribute__((ext_vector_type(2))) __bf16 bf16x2;
typedef __attribute__((ext_vector_type(4))) float f32x4;
typedef __attribute__((ext_vector_type(2))) float f32x2;
typedef __attribute__((ext_vector_type(4))) unsigned u32x4;
typedef __attribute__((ext_vector_type(2))) unsigned u32x2;
typedef __attribute__((ext_vector_type(4))) short i16x4;

#define STAMP(i) do {} while (0)
#define STAMPR(i) do {} while (0)
#define STAMP_DUMP() do { __builtin_amdgcn_s_waitcnt(0xc07f);} while (0)
#define SB() __builtin_amdgcn_sched_barrier(0)
#define LDMA(x) x
#define DSTAMP(i) do {} while (0)

namespace {

constexpr int SQ = 48, DM = 512, SK = 2048, DH = 128, NQ = 4096;

constexpr unsigned SLOT = 32768;
constexpr unsigned PB = 131072;
constexpr unsigned MISC = 155648;
constexpr unsigned MX_OFF = MISC;
constexpr unsigned LS_OFF = MISC + 1536;
constexpr unsigned FL_OFF = MISC + 3072;
constexpr unsigned XIMG = 98304;
constexpr unsigned LDS_BYTES = 163840;

constexpr size_t WS_EPOCH = 0;
constexpr size_t WS_IN1 = 4096;
constexpr size_t WS_IN2 = WS_IN1 + (size_t)256 * 32768;

constexpr float SCL = 0.08838834764831845f * 1.4426950408889634f;
constexpr unsigned SUM_CAP = 0x47800000u;
constexpr unsigned SPIN_CAP = 1u << 16;

__device__ __forceinline__ u32x4 lds_ld128(unsigned a) { return *(const AS3 u32x4*)(size_t)a; }
__device__ __forceinline__ void lds_st128(unsigned a, u32x4 v) { *(AS3 u32x4*)(size_t)a = v; }
__device__ __forceinline__ float lds_ldf(unsigned a) { return *(const AS3 float*)(size_t)a; }
__device__ __forceinline__ void lds_stf(unsigned a, float v) { *(AS3 float*)(size_t)a = v; }
__device__ __forceinline__ unsigned lds_ldu(unsigned a) { return *(const AS3 unsigned*)(size_t)a; }
__device__ __forceinline__ void lds_stu(unsigned a, unsigned v) { *(AS3 unsigned*)(size_t)a = v; }
__device__ __forceinline__ u32x2 lds_tr(unsigned a) {
  i16x4 r = __builtin_amdgcn_ds_read_tr16_b64_v4i16((AS3 i16x4*)(size_t)a);
  return __builtin_bit_cast(u32x2, r);
}
__device__ __forceinline__ f32x4 mfma(u32x4 a, u32x4 b, f32x4 c) {
  return __builtin_amdgcn_mfma_f32_16x16x32_bf16(__builtin_bit_cast(bf16x8, a), __builtin_bit_cast(bf16x8, b), c, 0, 0, 0);
}
__device__ __forceinline__ unsigned pk_bf16(float lo, float hi) {
  f32x2 v = {lo, hi};
  bf16x2 r = __builtin_convertvector(v, bf16x2);
  return __builtin_bit_cast(unsigned, r);
}
__device__ __forceinline__ float u2f(unsigned u) { return __builtin_bit_cast(float, u); }
__device__ __forceinline__ unsigned f2u(float f) { return __builtin_bit_cast(unsigned, f); }
__device__ __forceinline__ float bf_lo(unsigned u) { return __builtin_bit_cast(float, u << 16); }
__device__ __forceinline__ float bf_hi(unsigned u) { return __builtin_bit_cast(float, u & 0xffff0000u); }
__device__ __forceinline__ float ex2(float v) { return __builtin_amdgcn_exp2f(v); }

template <int OFF>
__device__ __forceinline__ void dma_nt(unsigned voff, const void* sbase, unsigned m0v) {
  asm volatile("s_mov_b32 m0, %2\n\ts_nop 0\n\tglobal_load_lds_dwordx4 %0, %1 offset:%3 nt"
               :: "v"(voff), "s"(sbase), "s"(m0v), "n"(OFF) : "memory");
}
template <int OFF>
__device__ __forceinline__ void dma_def(unsigned voff, const void* sbase, unsigned m0v) {
  asm volatile("s_mov_b32 m0, %2\n\ts_nop 0\n\tglobal_load_lds_dwordx4 %0, %1 offset:%3"
               :: "v"(voff), "s"(sbase), "s"(m0v), "n"(OFF) : "memory");
}
__device__ __forceinline__ void set_m0(unsigned m0v) { asm volatile("s_mov_b32 m0, %0\n\ts_nop 0" ::"s"(m0v) : "memory"); }
template <int OFF>
__device__ __forceinline__ void dma_0(unsigned vb, const void* sbase) {
  asm volatile("global_load_lds_dwordx4 %0, %1 offset:%2 nt" ::"v"(vb), "s"(sbase), "n"(OFF) : "memory");
}
template <int I, int N, class F>
__device__ __forceinline__ void static_for(F&& f) {
  if constexpr (I < N) {
    f(std::integral_constant<int, I>{});
    static_for<I + 1, N>(f);
  }
}
template <int N>
__device__ __forceinline__ void wait_barrier() {
  asm volatile("s_waitcnt vmcnt(%0) lgkmcnt(0)\n\ts_barrier" ::"n"(N) : "memory");
}
__device__ __forceinline__ void lds_barrier() { asm volatile("s_waitcnt lgkmcnt(0)\n\ts_barrier" ::: "memory"); }

__device__ __forceinline__ float grp_max(float v) {
  unsigned u = __builtin_bit_cast(unsigned, v);
  auto a = __builtin_amdgcn_permlane16_swap(u, u, false, false);
  v = fmaxf(u2f(a[0]), u2f(a[1]));
  u = __builtin_bit_cast(unsigned, v);
  auto c = __builtin_amdgcn_permlane32_swap(u, u, false, false);
  return fmaxf(u2f(c[0]), u2f(c[1]));
}
__device__ __forceinline__ float half_sum(float v) {
  unsigned u = __builtin_bit_cast(unsigned, v);
  auto c = __builtin_amdgcn_permlane32_swap(u, u, false, false);
  return u2f(c[0]) + u2f(c[1]);
}

__device__ __forceinline__ unsigned rotl5(unsigned h) { return __builtin_amdgcn_alignbit(h, h, 27); }
__device__ __forceinline__ void hash13(const u32x4& a, const u32x4& b, const u32x4& c, unsigned d, unsigned tag,
                                       unsigned& h1, unsigned& h2) {
  unsigned x = tag ^ 0x9E3779B9u, y = ~tag;
#pragma unroll
  for (int k = 0; k < 4; ++k) { x = (rotl5(x) ^ a[k]) + 0x85EBCA6Bu; }
#pragma unroll
  for (int k = 0; k < 2; ++k) { x = (rotl5(x) ^ b[k]) + 0x85EBCA6Bu; }
#pragma unroll
  for (int k = 2; k < 4; ++k) { y = (rotl5(y) ^ b[k]) + 0xC2B2AE35u; }
#pragma unroll
  for (int k = 0; k < 4; ++k) { y = (rotl5(y) ^ c[k]) + 0xC2B2AE35u; }
  y = (rotl5(y) ^ d) + 0xC2B2AE35u;
  h1 = (x ^ rotl5(y)) + y;
  h2 = (a[0] + a[1] + a[2]) + (a[3] + b[0] + b[1]) + (b[2] + b[3] + c[0]) + (c[1] + c[2] + c[3]) + (d + 2u * tag + 1u);
}

template <int STR>
__device__ __forceinline__ void recv4(const __amdgpu_buffer_rsrc_t& rs, unsigned src, unsigned tag, u32x4 (&th)[4]) {
  for (unsigned spin = 0; spin < 64; ++spin) {
    asm volatile("" ::: "memory");
#pragma unroll
    for (int k = 0; k < 4; ++k) th[k] = __builtin_amdgcn_raw_buffer_load_b128(rs, src + k * STR, 0, 16);
    unsigned h1, h2;
    hash13(th[0], th[1], th[2], th[3][0], tag, h1, h2);
    const bool ok = (h1 == th[3][1]) & (h2 == th[3][2]);
    if (__builtin_amdgcn_ballot_w64(ok) == ~0ull) break;
    for (unsigned s2 = 0; s2 < SPIN_CAP; ++s2) {
      asm volatile("" ::: "memory");
      const unsigned tg = __builtin_amdgcn_raw_buffer_load_b32(rs, src + 3 * STR + 12, 0, 16);
      if (__builtin_amdgcn_ballot_w64(tg == tag) == ~0ull) break;
      __builtin_amdgcn_s_sleep(2);
    }
  }
}

__global__ __launch_bounds__(512, 2) void gqa_fused_kernel(const uint16_t* __restrict__ x, const uint16_t* __restrict__ kc,
                                                           const uint16_t* __restrict__ vc, const uint16_t* __restrict__ wq,
                                                           const uint16_t* __restrict__ wo, uint16_t* __restrict__ out,
                                                           unsigned char* __restrict__ ws) {
  const int tid = threadIdx.x;
  const int lane = tid & 63;
  const int w = __builtin_amdgcn_readfirstlane(tid >> 6);
  const int bid = blockIdx.x;
  const int kvh = bid & 7, slot = bid >> 3, hp = slot & 1, kh = (slot >> 1) & 1, b = slot >> 2;
  const int g = lane >> 4, i16 = lane & 15;
  const int hh = w >> 2, jq = w & 3;
  const int head0 = kvh * 4 + hp * 2;

  STAMPR(0);
  unsigned* epoch_ptr = (unsigned*)(ws + WS_EPOCH + (size_t)b * 256);
  const unsigned epoch_v = __hip_atomic_load(epoch_ptr, __ATOMIC_RELAXED, __HIP_MEMORY_SCOPE_AGENT);

  const char* kbase = (const char*)kc + ((size_t)(b * 8 + kvh) * SK + kh * 1024) * (DH * 2);
  const char* vbase = (const char*)vc + ((size_t)(b * 8 + kvh) * SK + kh * 1024) * (DH * 2);
  const char* wobase = (const char*)wo + ((size_t)head0 * DH * DM + kh * 256) * 2;

  unsigned voffK[4], voffV[2];
#pragma unroll
  for (int i = 0; i < 4; ++i) voffK[i] = (unsigned)(w * 4096 + g * 256 + ((i16 ^ (4 * i + g) ^ ((i & 1) << 3)) << 4));
#pragma unroll
  for (int i = 0; i < 2; ++i) voffV[i] = (unsigned)(w * 4096 + g * 256 + ((i16 ^ (2 * (4 * i + g))) << 4));
  const unsigned w4k = (unsigned)(w * 4096);
  u32x4 qf[4][3];

  if (w < 4) {
    const char* xb = (const char*)x + (size_t)b * SQ * DM * 2;
    static_for<0, 12>([&](auto rc) {
      constexpr int r = decltype(rc)::value;
      const int row = w * 12 + r;
      dma_def<0>((unsigned)((lane ^ (row & 15)) << 4), xb + row * 1024, (unsigned)(XIMG + row * 1024));
    });
    auto item = [&](auto n0c, auto n1c) {
      static_for<decltype(n0c)::value, decltype(n1c)::value>([&](auto nc) {
        constexpr int n = decltype(nc)::value, it = n >> 3, vw = (n >> 2) & 1, i = n & 3;
        const char* src = (it == 2 ? vbase : kbase + it * 32768) + vw * 16384;
        dma_nt<i * 1024>(it == 2 ? voffV[i & 1] : voffK[i], src, w4k + it * SLOT + vw * 16384);
        __builtin_amdgcn_s_sleep(2);
      });
    };
    item(std::integral_constant<int, 0>{}, std::integral_constant<int, 6>{});
    if (lane < 16) lds_stu(FL_OFF + lane * 4, 0u);
    wait_barrier<6>();
    item(std::integral_constant<int, 6>{}, std::integral_constant<int, 24>{});
    lds_barrier();
    lds_barrier();
    lds_barrier();
  } else {
    const int idx = w - 4, qh = idx >> 1, khalf = idx & 1;
    const __amdgpu_buffer_rsrc_t wq_rs = __builtin_amdgcn_make_buffer_rsrc((void*)wq, 0, DM * NQ * 2, 0x00020000);
    const unsigned wq_voff = (unsigned)((256 * khalf + 8 * g) * (NQ * 2) + (head0 + qh) * (DH * 2) + i16 * 16);
    constexpr int PRE = 3;
    u32x4 wr[8][8];
#pragma unroll
    for (int ks = 0; ks < PRE; ++ks)
#pragma unroll
      for (int j = 0; j < 8; ++j) wr[ks][j] = __builtin_amdgcn_raw_buffer_load_b128(wq_rs, wq_voff, (32 * ks + j) * (NQ * 2), 0);
    wait_barrier<63>();
    STAMP(29);
    f32x4 acc[8][3];
#pragma unroll
    for (int e = 0; e < 8; ++e)
#pragma unroll
      for (int t = 0; t < 3; ++t) acc[e][t] = f32x4{0.f, 0.f, 0.f, 0.f};
    unsigned xa[4];
#pragma unroll
    for (int k = 0; k < 4; ++k) xa[k] = XIMG + i16 * 1024 + khalf * 512 + (((4 * k + g) ^ i16) << 4);
#pragma unroll
    for (int ks = 0; ks < 8; ++ks) {
      u32x4 xf[3];
#pragma unroll
      for (int t = 0; t < 3; ++t) xf[t] = lds_ld128(xa[ks & 3] + (ks >> 2) * 256 + t * 16384);
#pragma unroll
      for (int e = 0; e < 8; ++e) {
        u32x4 af;
#pragma unroll
        for (int r = 0; r < 4; ++r)
          af[r] = __builtin_amdgcn_perm(wr[ks][2 * r + 1][e >> 1], wr[ks][2 * r][e >> 1], (e & 1) ? 0x07060302u : 0x05040100u);
#pragma unroll
        for (int t = 0; t < 3; ++t) acc[e][t] = mfma(af, xf[t], acc[e][t]);
      }
      SB();
      if (ks + PRE < 8) {
#pragma unroll
        for (int j = 0; j < 8; ++j)
          wr[ks + PRE][j] = __builtin_amdgcn_raw_buffer_load_b128(wq_rs, wq_voff, (32 * (ks + PRE) + j) * (NQ * 2), 0);
      }
      SB();
    }
    STAMP(30);
    lds_barrier();
    {
      const unsigned snd = XIMG + (idx ^ 1) * 12288 + lane * 16;
#pragma unroll
      for (int t = 0; t < 3; ++t)
#pragma unroll
        for (int rr = 0; rr < 2; ++rr) {
          f32x4 lo, hi;
#pragma unroll
          for (int e = 0; e < 4; ++e) {
            lo[e] = khalf ? acc[e][t][rr] : acc[e][t][2 + rr];
            hi[e] = khalf ? acc[4 + e][t][rr] : acc[4 + e][t][2 + rr];
          }
          lds_st128(snd + (t * 2 + rr) * 2048, __builtin_bit_cast(u32x4, lo));
          lds_st128(snd + (t * 2 + rr) * 2048 + 1024, __builtin_bit_cast(u32x4, hi));
        }
    }
    lds_barrier();
    {
      const unsigned rcv = XIMG + idx * 12288 + lane * 16;
      f32x4 plo[3][2], phi[3][2];
#pragma unroll
      for (int t = 0; t < 3; ++t)
#pragma unroll
        for (int rr = 0; rr < 2; ++rr) {
          plo[t][rr] = __builtin_bit_cast(f32x4, lds_ld128(rcv + (t * 2 + rr) * 2048));
          phi[t][rr] = __builtin_bit_cast(f32x4, lds_ld128(rcv + (t * 2 + rr) * 2048 + 1024));
        }
#pragma unroll
      for (int t = 0; t < 3; ++t)
#pragma unroll
        for (int rr = 0; rr < 2; ++rr) {
          float v[8];
#pragma unroll
          for (int e = 0; e < 4; ++e) {
            v[e] = ((khalf ? acc[e][t][2 + rr] : acc[e][t][rr]) + plo[t][rr][e]) * SCL;
            v[4 + e] = ((khalf ? acc[4 + e][t][2 + rr] : acc[4 + e][t][rr]) + phi[t][rr][e]) * SCL;
          }
          lds_st128(rcv + (rr * 3 + t) * 1024, u32x4{pk_bf16(v[0], v[1]), pk_bf16(v[2], v[3]), pk_bf16(v[4], v[5]), pk_bf16(v[6], v[7])});
        }
    }
    STAMP(31);
    lds_barrier();
  }
#pragma unroll
  for (int s = 0; s < 4; ++s)
#pragma unroll
    for (int t = 0; t < 3; ++t) qf[s][t] = lds_ld128(XIMG + (hh * 2 + (s >> 1)) * 12288 + ((s & 1) * 3 + t) * 1024 + lane * 16);
  STAMPR(1);

  unsigned ka[4];
#pragma unroll
  for (int s = 0; s < 4; ++s) ka[s] = (32 * jq + i16) * 256 + (((4 * g + s) ^ i16 ^ ((i16 & 4) << 1)) << 4);
  unsigned va[4];
  {
    const int q_ = (lane >> 2) & 3, p = lane & 3;
#pragma unroll
    for (int dt = 0; dt < 4; ++dt)
      va[dt] = 2 * SLOT + (32 * jq + 4 * g + q_) * 256 + (((8 * (jq & 1) + 2 * dt + (p >> 1)) ^ (2 * ((4 * g + q_) & 7))) << 4) + 8 * (p & 1);
  }
  const unsigned pw = PB + hh * 12288 + (jq >> 1) * 6144 + (jq & 1) * 1024 + lane * 16;

  f32x4 O[4][3];
#pragma unroll
  for (int dt = 0; dt < 4; ++dt)
#pragma unroll
    for (int t = 0; t < 3; ++t) O[dt][t] = f32x4{0.f, 0.f, 0.f, 0.f};
  float lsum[3] = {0.f, 0.f, 0.f};
  f32x4 nm[3];
  f32x4 ps[3];
  const u32x4 ones = {0x3F803F80u, 0x3F803F80u, 0x3F803F80u, 0x3F803F80u};
  const f32x4 zero4 = {0.f, 0.f, 0.f, 0.f};
  const u32x4 zerou = {0u, 0u, 0u, 0u};

  auto tile_max = [&](const f32x4(&S)[2][3], int t) {
    float m = fmaxf(fmaxf(S[0][t][0], S[0][t][1]), S[0][t][2]);
    m = fmaxf(fmaxf(m, S[0][t][3]), S[1][t][0]);
    m = fmaxf(fmaxf(m, S[1][t][1]), S[1][t][2]);
    return fmaxf(m, S[1][t][3]);
  };
  auto head_max = [&](const f32x4(&S)[2][3], float (&m)[3]) {
#pragma unroll
    for (int t = 0; t < 3; ++t) {
      const float v = grp_max(tile_max(S, t));
      if (lane < 16) lds_stf(MX_OFF + ((hh * 4 + jq) * 3 + t) * 64 + lane * 4, v);
    }
    lds_barrier();
#pragma unroll
    for (int t = 0; t < 3; ++t) {
      float v = lds_ldf(MX_OFF + ((hh * 4 + 0) * 3 + t) * 64 + i16 * 4);
#pragma unroll
      for (int j = 1; j < 4; ++j) v = fmaxf(v, lds_ldf(MX_OFF + ((hh * 4 + j) * 3 + t) * 64 + i16 * 4));
      m[t] = v;
    }
  };
  auto p_unit = [&](const f32x4& s) { return u32x2{pk_bf16(ex2(s[0]), ex2(s[1])), pk_bf16(ex2(s[2]), ex2(s[3]))}; };
  auto p_put = [&](int t, u32x2 a, u32x2 c) {
    const u32x4 f = {a[0], a[1], c[0], c[1]};
    lds_st128(pw + t * 2048, f);
    return f;
  };
  auto v_frag = [&](unsigned a) {
    const u32x2 v0 = lds_tr(a), v1 = lds_tr(a + 4096);
    return u32x4{v0[0], v0[1], v1[0], v1[1]};
  };

  unsigned vxb = voffV[0], vyb = voffK[0];
  unsigned wvb = (unsigned)((16 * w + g) * 1024 + ((i16 ^ (2 * g)) << 4));
#define IC(v) std::integral_constant<int, (v)>{}

  f32x4 SA[2][3], SB_[2][3];
  u32x4 Pown[3], Ppar[3], vg[4], kf[8];
#pragma unroll
  for (int t = 0; t < 3; ++t) Ppar[t] = zerou;
  vg[2] = zerou;
  vg[3] = zerou;
  wait_barrier<16>();
  STAMP(2);
#pragma unroll
  for (int kt = 0; kt < 2; ++kt)
#pragma unroll
    for (int s = 0; s < 4; ++s) {
      const u32x4 kf0 = lds_ld128(ka[s] + kt * 4096);
#pragma unroll
      for (int t = 0; t < 3; ++t) SA[kt][t] = mfma(kf0, qf[s][t], s == 0 ? zero4 : SA[kt][t]);
    }
  {
    float m[3];
    head_max(SA, m);
    static_for<0, 4>([&](auto ic) { constexpr int i = decltype(ic)::value; dma_nt<i * 1024>(voffK[i], kbase + 2 * 32768, w4k); });
#pragma unroll
    for (int t = 0; t < 3; ++t) {
      nm[t] = f32x4{-m[t], -m[t], -m[t], -m[t]};
      SA[0][t] += nm[t];
      SA[1][t] += nm[t];
    }
  }

  struct Req { const char* src; unsigned ex, x1, x2, x3, vb; };
  auto req_issue = [&](auto ic, const Req& r) {
    constexpr int i = decltype(ic)::value;
    if constexpr (i == 0) dma_0<0>(r.vb, r.src);
    else dma_0<i * 1024>(r.vb ^ (i == 1 ? r.x1 : (i == 2 ? r.x2 : r.x3)), r.src + i * r.ex);
  };
  auto pv3 = [&](int dt, const u32x4& v, const u32x4 (&p)[3]) {
#pragma unroll
    for (int t = 0; t < 3; ++t) O[dt][t] = mfma(v, p[t], O[dt][t]);
  };
  auto xphase = [&](auto ksc, auto dsc, auto lastc, f32x4(&SC)[2][3], f32x4(&SN)[2][3], const Req& rq, int t) {
    constexpr unsigned ko = decltype(ksc)::value * SLOT;
    constexpr bool LAST = decltype(lastc)::value;
    LDMA(set_m0(w4k + decltype(dsc)::value * SLOT));
    auto qk = [&](auto cc) {
      constexpr int c = decltype(cc)::value, kt = c >> 2, s = c & 3;
      if constexpr (!LAST) {
#pragma unroll
        for (int tt = 0; tt < 3; ++tt) SN[kt][tt] = mfma(kf[c], qf[s][tt], s == 0 ? nm[tt] : SN[kt][tt]);
      }
    };
    if constexpr (!LAST) {
#pragma unroll
      for (int c = 0; c < 4; ++c) kf[c] = lds_ld128(ka[c] + ko);
    }
    SB();
    pv3(2, vg[2], Ppar);
    u32x2 a0 = p_unit(SC[0][0]);
    SB();
    pv3(3, vg[3], Ppar);
    u32x2 a1 = p_unit(SC[1][0]);
    Pown[0] = p_put(0, a0, a1);
    SB();
    if constexpr (!LAST) {
#pragma unroll
      for (int c = 4; c < 8; ++c) kf[c] = lds_ld128(ka[c & 3] + ko + 4096);
    }
    LDMA(req_issue(IC(0), rq));
    SB();
    qk(IC(0));
    a0 = p_unit(SC[0][1]);
    SB();
    qk(IC(1));
    a1 = p_unit(SC[1][1]);
    Pown[1] = p_put(1, a0, a1);
    ps[0] = mfma(ones, Pown[0], zero4);
    SB();
    LDMA(req_issue(IC(1), rq));
    SB();
    qk(IC(2));
    a0 = p_unit(SC[0][2]);
    SB();
    qk(IC(3));
    a1 = p_unit(SC[1][2]);
    Pown[2] = p_put(2, a0, a1);
    ps[1] = mfma(ones, Pown[1], zero4);
    SB();
    LDMA(req_issue(IC(2), rq));
    SB();
    qk(IC(4));
    ps[2] = mfma(ones, Pown[2], zero4);
    SB();
    LDMA(req_issue(IC(3), rq));
    SB();
    qk(IC(5));
    {
      const unsigned u0 = f2u(ps[0][0]), u1 = f2u(ps[1][0]), u2 = f2u(ps[2][0]);
      const unsigned um = max(max(u0, u1), u2);
      if (um > SUM_CAP) lds_stu(FL_OFF + t * 4, 1u);
    }
    SB();
  };
  auto yphase = [&](auto vsc, auto dsc, auto lastc, f32x4(&SC)[2][3], f32x4(&SN)[2][3], const Req& rq, int t) {
    constexpr unsigned vo = (decltype(vsc)::value - 2) * SLOT;
    constexpr bool LAST = decltype(lastc)::value;
    if constexpr (!LAST) LDMA(set_m0(w4k + decltype(dsc)::value * SLOT));
    const unsigned flagv = lds_ldu(FL_OFF + t * 4);
    SB();
    u32x4 vf[4];
#pragma unroll
    for (int dt = 0; dt < 4; ++dt) vf[dt] = v_frag(va[dt] + vo);
    SB();
    if constexpr (!LAST) {
#pragma unroll
      for (int tt = 0; tt < 3; ++tt) SN[1][tt] = mfma(kf[6], qf[2][tt], SN[1][tt]);
#pragma unroll
      for (int tt = 0; tt < 3; ++tt) SN[1][tt] = mfma(kf[7], qf[3][tt], SN[1][tt]);
    }
    SB();
    if (__builtin_amdgcn_readfirstlane(flagv)) {
      float m[3];
      head_max(SC, m);
#pragma unroll
      for (int tt = 0; tt < 3; ++tt) {
        const float d = fmaxf(m[tt], 0.f);
        const float alpha = ex2(-d);
        const f32x4 d4 = {d, d, d, d};
        lsum[tt] *= alpha;
#pragma unroll
        for (int dt = 0; dt < 4; ++dt) O[dt][tt] *= alpha;
        nm[tt] -= d4;
        SC[0][tt] -= d4;
        SC[1][tt] -= d4;
        if constexpr (!LAST) { SN[0][tt] -= d4; SN[1][tt] -= d4; }
        Pown[tt] = p_put(tt, p_unit(SC[0][tt]), p_unit(SC[1][tt]));
        ps[tt] = mfma(ones, Pown[tt], zero4);
      }
      lds_barrier();
    }
    SB();
#pragma unroll
    for (int tt = 0; tt < 3; ++tt) Ppar[tt] = lds_ld128((pw ^ 1024u) + tt * 2048);
#pragma unroll
    for (int dt = 0; dt < 4; ++dt) vg[dt] = v_frag((va[dt] ^ 8192u) + vo);
#pragma unroll
    for (int tt = 0; tt < 3; ++tt) lsum[tt] += ps[tt][0];
    SB();
    pv3(0, vf[0], Pown);
    if constexpr (!LAST) LDMA(req_issue(IC(0), rq));
    SB();
    pv3(1, vf[1], Pown);
    if constexpr (!LAST) LDMA(req_issue(IC(1), rq));
    SB();
    pv3(2, vf[2], Pown);
    if constexpr (!LAST) LDMA(req_issue(IC(2), rq));
    SB();
    pv3(3, vf[3], Pown);
    if constexpr (!LAST) LDMA(req_issue(IC(3), rq));
    SB();
    pv3(0, vg[0], Ppar);
    pv3(1, vg[1], Ppar);
    if constexpr (LAST) {
      pv3(2, vg[2], Ppar);
      pv3(3, vg[3], Ppar);
    }
    SB();
  };
  auto wsrc = [&](int wi) { return wobase + (size_t)(wi >> 1) * 128 * 1024 + (wi & 1) * 256; };
  #define LAUNDER(v) asm volatile("" : "+v"(v))
  auto reqV = [&](int t) { LAUNDER(vxb); return Req{vbase + (size_t)t * 32768, 0u, 0x80u, 0u, 0x80u, vxb}; };
  auto reqK = [&](int t) { LAUNDER(vyb); return Req{kbase + (size_t)t * 32768, 0u, 0xC0u, 0x80u, 0x40u, vyb}; };
  auto reqW = [&](int wi) { LAUNDER(wvb); return Req{wsrc(wi), 3072u, 0x80u, 0u, 0x80u, wvb}; };

#pragma unroll 1
  for (int t2 = 0; t2 < 6; t2 += 2) {
    wait_barrier<8>();
    STAMP(3 + 2 * t2);
    xphase(IC(1), IC(3), std::false_type{}, SA, SB_, reqV(t2 + 1), t2);
    DSTAMP(25);
    wait_barrier<8>();
    STAMP(4 + 2 * t2);
    yphase(IC(2), IC(1), std::false_type{}, SA, SB_, reqK(t2 + 3), t2);
    DSTAMP(26);
    wait_barrier<8>();
    STAMP(5 + 2 * t2);
    xphase(IC(0), IC(2), std::false_type{}, SB_, SA, reqV(t2 + 2), t2 + 1);
    const Req ry = t2 == 4 ? reqW(0) : reqK(t2 + 4);
    DSTAMP(27);
    wait_barrier<8>();
    STAMP(6 + 2 * t2);
    yphase(IC(3), IC(0), std::false_type{}, SB_, SA, ry, t2 + 1);
    DSTAMP(28);
  }
  wait_barrier<8>();
  STAMP(15);
  xphase(IC(1), IC(3), std::false_type{}, SA, SB_, reqV(7), 6);
  wait_barrier<8>();
  STAMP(16);
  yphase(IC(2), IC(1), std::false_type{}, SA, SB_, reqW(1), 6);
  wait_barrier<8>();
  STAMP(17);
  xphase(IC(0), IC(2), std::true_type{}, SB_, SA, reqW(2), 7);
  wait_barrier<8>();
  STAMP(18);
  yphase(IC(3), IC(3), std::true_type{}, SB_, SA, reqW(3), 7);

#pragma unroll
  for (int t = 0; t < 3; ++t)
    if (lane < 16) lds_stf(LS_OFF + ((hh * 4 + jq) * 3 + t) * 64 + lane * 4, lsum[t]);
  lds_barrier();
  STAMPR(19);
  f32x4 Om[2][3];
  {
    const int jk = jq >> 1;
    const unsigned snd = XIMG + (w ^ 2) * 6144 + lane * 16, rcv = XIMG + w * 6144 + lane * 16;
#pragma unroll
    for (int dd = 0; dd < 2; ++dd)
#pragma unroll
      for (int t = 0; t < 3; ++t) lds_st128(snd + (dd * 3 + t) * 1024, __builtin_bit_cast(u32x4, jk ? O[dd][t] : O[2 + dd][t]));
    lds_barrier();
#pragma unroll
    for (int dd = 0; dd < 2; ++dd)
#pragma unroll
      for (int t = 0; t < 3; ++t) Om[dd][t] = (jk ? O[2 + dd][t] : O[dd][t]) + __builtin_bit_cast(f32x4, lds_ld128(rcv + (dd * 3 + t) * 1024));
    lds_barrier();
  }
  float lt[3];
#pragma unroll
  for (int t = 0; t < 3; ++t) {
    float s = 0.f;
#pragma unroll
    for (int j = 0; j < 4; ++j) s += lds_ldf(LS_OFF + ((hh * 4 + j) * 3 + t) * 64 + i16 * 4);
    lt[t] = s;
  }

  const unsigned tag = (unsigned)__builtin_amdgcn_readfirstlane(epoch_v) + 1u;
  const __amdgpu_buffer_rsrc_t ws_rs = __builtin_amdgcn_make_buffer_rsrc((void*)ws, 0, (unsigned)(WS_IN2 + (size_t)256 * 32768), 0x00020000);

  const unsigned ow = PB + hh * 12288 + (2 * (jq & 1) + (jq >> 1)) * 3072 + lane * 16;
  float lam[3];
  u32x4 mine[3];
#pragma unroll
  for (int t = 0; t < 3; ++t) {
    const float inv = 1.0f / lt[t];
    lam[t] = __builtin_amdgcn_logf(lt[t]) - nm[t][0];
#pragma unroll
    for (int dt = 0; dt < 2; ++dt) Om[dt][t] *= inv;
    mine[t] = u32x4{pk_bf16(Om[0][t][0], Om[0][t][1]), pk_bf16(Om[0][t][2], Om[0][t][3]), pk_bf16(Om[1][t][0], Om[1][t][1]),
                    pk_bf16(Om[1][t][2], Om[1][t][3])};
  }
  {
    const float l0 = lam[0], l1 = lam[1], l2 = lam[2];
    const unsigned lw = __builtin_bit_cast(unsigned, (lane & 32) ? l2 : ((lane & 16) ? l1 : l0));
    unsigned h1, h2;
    hash13(mine[0], mine[1], mine[2], lw, tag, h1, h2);
    const unsigned dst = (unsigned)(WS_IN1 + (size_t)(bid ^ 16) * 32768 + w * 4096 + lane * 16);
    __builtin_amdgcn_raw_buffer_store_b128(mine[0], ws_rs, dst, 0, 16);
    __builtin_amdgcn_raw_buffer_store_b128(mine[1], ws_rs, dst + 1024, 0, 16);
    __builtin_amdgcn_raw_buffer_store_b128(mine[2], ws_rs, dst + 2048, 0, 16);
    __builtin_amdgcn_raw_buffer_store_b128(u32x4{lw, h1, h2, tag}, ws_rs, dst + 3072, 0, 16);
  }
  set_m0(w4k + 3 * SLOT);
  static_for<0, 4>([&](auto ic) { req_issue(ic, reqW(3)); });
  u32x4 th[4];
  recv4<1024>(ws_rs, (unsigned)(WS_IN1 + (size_t)bid * 32768 + w * 4096 + lane * 16), tag, th);
  STAMPR(20);
  {
    const unsigned u = th[3][0];
    auto a = __builtin_amdgcn_permlane32_swap(u, u, false, false);
    auto c0 = __builtin_amdgcn_permlane16_swap(a[0], a[0], false, false);
    auto c1 = __builtin_amdgcn_permlane16_swap(a[1], a[1], false, false);
    const float lb[3] = {u2f(c0[0]), u2f(c0[1]), u2f(c1[0])};
#pragma unroll
    for (int t = 0; t < 3; ++t) {
      const float mx = fmaxf(lam[t], lb[t]);
      float wa = ex2(lam[t] - mx), wb = ex2(lb[t] - mx);
      const float inv = 1.0f / (wa + wb);
      wa *= inv;
      wb *= inv;
      float m[8];
#pragma unroll
      for (int k = 0; k < 4; ++k) {
        m[2 * k] = wa * Om[k >> 1][t][2 * (k & 1)] + wb * bf_lo(th[t][k]);
        m[2 * k + 1] = wa * Om[k >> 1][t][2 * (k & 1) + 1] + wb * bf_hi(th[t][k]);
      }
      lds_st128(ow + t * 1024, u32x4{pk_bf16(m[0], m[1]), pk_bf16(m[2], m[3]), pk_bf16(m[4], m[5]), pk_bf16(m[6], m[7])});
    }
  }
  wait_barrier<0>();
  STAMP(21);

  unsigned wa_[2];
  {
    const int q_ = (lane >> 2) & 3, p = lane & 3;
#pragma unroll
    for (int dt = 0; dt < 2; ++dt)
      wa_[dt] = (4 * g + q_) * 256 + (((4 * jq + 2 * dt + (p >> 1)) ^ (2 * ((4 * g + q_) & 7))) << 4) + 8 * (p & 1);
  }
  const unsigned pr = PB + lane * 16;
  auto pv_load = [&](unsigned soff, unsigned poff, int ks, u32x4 (&pf)[3], u32x4 (&vf)[2]) {
#pragma unroll
    for (int t = 0; t < 3; ++t) pf[t] = lds_ld128(pr + poff + ks * 3072 + t * 1024);
#pragma unroll
    for (int dt = 0; dt < 2; ++dt) vf[dt] = v_frag(wa_[dt] + soff + ks * 8192);
  };
  auto pv_mma = [&](const u32x4 (&pf)[3], const u32x4 (&vf)[2], f32x4(&A)[2][3]) {
#pragma unroll
    for (int dt = 0; dt < 2; ++dt)
#pragma unroll
      for (int t = 0; t < 3; ++t) A[dt][t] = mfma(vf[dt], pf[t], A[dt][t]);
  };
  f32x4 A[2][3];
#pragma unroll
  for (int dt = 0; dt < 2; ++dt)
#pragma unroll
    for (int t = 0; t < 3; ++t) A[dt][t] = zero4;
  {
    u32x4 pf0[3], vf0[2], pf1[3], vf1[2];
    pv_load((0 + hh) * SLOT, 0, 0, pf0, vf0);
    pv_load((0 + hh) * SLOT, 0, 1, pf1, vf1);
#pragma unroll
    for (int st = 0; st < 8; st += 2) {
      pv_mma(pf0, vf0, A);
      if (st + 2 < 8) pv_load((2 * ((st + 2) >> 2) + hh) * SLOT, ((st + 2) >> 2) * 12288, (st + 2) & 3, pf0, vf0);
      pv_mma(pf1, vf1, A);
      if (st + 3 < 8) pv_load((2 * ((st + 3) >> 2) + hh) * SLOT, ((st + 3) >> 2) * 12288, (st + 3) & 3, pf1, vf1);
    }
  }
  lds_barrier();
  STAMP(22);

  {
    u32x4 f[3];
#pragma unroll
    for (int t = 0; t < 3; ++t)
      f[t] = u32x4{pk_bf16(A[0][t][0], A[0][t][1]), pk_bf16(A[0][t][2], A[0][t][3]), pk_bf16(A[1][t][0], A[1][t][1]),
                   pk_bf16(A[1][t][2], A[1][t][3])};
    unsigned h1, h2;
    hash13(f[0], f[1], f[2], 0u, tag, h1, h2);
    const int rho = 2 * w + (i16 >> 3);
    const int rbid = (rho >> 1) + 8 * ((rho & 1) + 2 * kh + 4 * b);
    const int pidx = kvh * 2 + hp;
    const unsigned dst = (unsigned)(WS_IN2 + (size_t)rbid * 32768 + pidx * 2048 + (8 * g + (i16 & 7)) * 16);
    __builtin_amdgcn_raw_buffer_store_b128(f[0], ws_rs, dst, 0, 16);
    __builtin_amdgcn_raw_buffer_store_b128(f[1], ws_rs, dst + 512, 0, 16);
    __builtin_amdgcn_raw_buffer_store_b128(f[2], ws_rs, dst + 1024, 0, 16);
    __builtin_amdgcn_raw_buffer_store_b128(u32x4{0u, h1, h2, tag}, ws_rs, dst + 1536, 0, 16);
  }
  {
    recv4<512>(ws_rs, (unsigned)(WS_IN2 + (size_t)bid * 32768 + (tid >> 5) * 2048 + (tid & 31) * 16), tag, th);
    STAMPR(23);
#pragma unroll
    for (int t = 0; t < 3; ++t) {
      f32x4 r;
#pragma unroll
      for (int k = 0; k < 2; ++k) {
        const float a0 = half_sum(bf_lo(th[t][k])), a1 = half_sum(bf_hi(th[t][k]));
        const float b0 = half_sum(bf_lo(th[t][2 + k])), b1 = half_sum(bf_hi(th[t][2 + k]));
        r[2 * k] = lane < 32 ? a0 : b0;
        r[2 * k + 1] = lane < 32 ? a1 : b1;
      }
      lds_st128(PB + w * 3072 + t * 1024 + lane * 16, __builtin_bit_cast(u32x4, r));
    }
  }
  lds_barrier();
  if (tid < 192) {
    const int t = tid >> 6;
    f32x4 s = __builtin_bit_cast(f32x4, lds_ld128(PB + t * 1024 + lane * 16));
#pragma unroll
    for (int v = 1; v < 8; ++v) s += __builtin_bit_cast(f32x4, lds_ld128(PB + v * 3072 + t * 1024 + lane * 16));
    const int rho = kvh * 2 + hp;
    const int nt = lane >> 5, l32 = lane & 31, gg = l32 >> 3, q = 8 * (rho & 1) + (l32 & 7);
    const int col = 256 * kh + 32 * (rho >> 1) + 16 * nt + 4 * gg;
    u32x2 o = {pk_bf16(s[0], s[1]), pk_bf16(s[2], s[3])};
    *(u32x2*)(out + ((size_t)b * SQ + 16 * t + q) * DM + col) = o;
  }
  STAMPR(24);
  STAMP_DUMP();
  if (tid == 0 && kvh == 0 && hp == 0 && kh == 0) __hip_atomic_store(epoch_ptr, tag, __ATOMIC_RELAXED, __HIP_MEMORY_SCOPE_AGENT);
}

}

extern "C" void kernel_launch(void* const* d_in, const int* in_sizes, int n_in, void* d_out, int out_size, void* d_ws,
                              size_t ws_size, hipStream_t stream) {
  (void)in_sizes; (void)n_in; (void)out_size; (void)ws_size;
  (void)hipFuncSetAttribute((const void*)gqa_fused_kernel, hipFuncAttributeMaxDynamicSharedMemorySize, LDS_BYTES);
  gqa_fused_kernel<<<256, 512, LDS_BYTES, stream>>>((const uint16_t*)d_in[0], (const uint16_t*)d_in[1], (const uint16_t*)d_in[2],
                                                    (const uint16_t*)d_in[3], (const uint16_t*)d_in[4], (uint16_t*)d_out,
                                                    (unsigned char*)d_ws);
}
```

```cpp
#include <hip/hip_runtime.h>
#include <hip/hip_bf16.h>
#include <stdint.h>
#include <type_traits>

#define AS3 __attribute__((address_space(3)))
typedef __attribute__((ext_vector_type(8))) __bf16 bf16x8;
typedef __attribute__((ext_vector_type(2))) __bf16 bf16x2;
typedef __attribute__((ext_vector_type(4))) float f32x4;
typedef __attribute__((ext_vector_type(2))) float f32x2;
typedef __attribute__((ext_vector_type(4))) unsigned u32x4;
typedef __attribute__((ext_vector_type(2))) unsigned u32x2;
typedef __attribute__((ext_vector_type(4))) short i16x4;

#define STAMP(i) do {} while (0)
#define STAMPR(i) do {} while (0)
#define STAMP_DUMP() do { __builtin_amdgcn_s_waitcnt(0xc07f);} while (0)
#define SB() __builtin_amdgcn_sched_barrier(0)
#define KVPOL " nt"
#define LDMA(x) x
#define DSTAMP(i) do {} while (0)

namespace {

constexpr int SQ = 48, DM = 512, SK = 2048, DH = 128, NQ = 4096;

constexpr unsigned SLOT = 32768;
constexpr unsigned PB = 131072;
constexpr unsigned MISC = 155648;
constexpr unsigned MX_OFF = MISC;
constexpr unsigned LS_OFF = MISC + 1536;
constexpr unsigned FL_OFF = MISC + 3072;
constexpr unsigned TOUCH_OFF = MISC + 3328;
constexpr unsigned XIMG = 98304;
constexpr unsigned LDS_BYTES = 163840;

constexpr unsigned WS_EPOCH = 0;
constexpr unsigned WS_HINT1 = 4096;
constexpr unsigned WS_HINT2 = WS_HINT1 + 32768;
constexpr unsigned WS_IN = 2u << 20;
constexpr unsigned WS_IN2 = 1u << 20;
constexpr unsigned WS_END = WS_IN * 9;

constexpr float SCL = 0.08838834764831845f * 1.4426950408889634f;
constexpr unsigned SUM_CAP = 0x47800000u;
constexpr unsigned SPIN_CAP = 1u << 16;

__device__ __forceinline__ u32x4 lds_ld128(unsigned a) { return *(const AS3 u32x4*)(size_t)a; }
__device__ __forceinline__ void lds_st128(unsigned a, u32x4 v) { *(AS3 u32x4*)(size_t)a = v; }
__device__ __forceinline__ float lds_ldf(unsigned a) { return *(const AS3 float*)(size_t)a; }
__device__ __forceinline__ void lds_stf(unsigned a, float v) { *(AS3 float*)(size_t)a = v; }
__device__ __forceinline__ unsigned lds_ldu(unsigned a) { return *(const AS3 unsigned*)(size_t)a; }
__device__ __forceinline__ void lds_stu(unsigned a, unsigned v) { *(AS3 unsigned*)(size_t)a = v; }
__device__ __forceinline__ u32x2 lds_tr(unsigned a) {
  i16x4 r = __builtin_amdgcn_ds_read_tr16_b64_v4i16((AS3 i16x4*)(size_t)a);
  return __builtin_bit_cast(u32x2, r);
}
__device__ __forceinline__ f32x4 mfma(u32x4 a, u32x4 b, f32x4 c) {
  return __builtin_amdgcn_mfma_f32_16x16x32_bf16(__builtin_bit_cast(bf16x8, a), __builtin_bit_cast(bf16x8, b), c, 0, 0, 0);
}
__device__ __forceinline__ unsigned pk_bf16(float lo, float hi) {
  f32x2 v = {lo, hi};
  bf16x2 r = __builtin_convertvector(v, bf16x2);
  return __builtin_bit_cast(unsigned, r);
}
__device__ __forceinline__ float u2f(unsigned u) { return __builtin_bit_cast(float, u); }
__device__ __forceinline__ unsigned f2u(float f) { return __builtin_bit_cast(unsigned, f); }
__device__ __forceinline__ float bf_lo(unsigned u) { return __builtin_bit_cast(float, u << 16); }
__device__ __forceinline__ float bf_hi(unsigned u) { return __builtin_bit_cast(float, u & 0xffff0000u); }
__device__ __forceinline__ float ex2(float v) { return __builtin_amdgcn_exp2f(v); }

template <int OFF>
__device__ __forceinline__ void dma_nt(unsigned voff, const void* sbase, unsigned m0v) {
  asm volatile("s_mov_b32 m0, %2\n\ts_nop 0\n\tglobal_load_lds_dwordx4 %0, %1 offset:%3" KVPOL
               :: "v"(voff), "s"(sbase), "s"(m0v), "n"(OFF) : "memory");
}
template <int OFF>
__device__ __forceinline__ void dma_def(unsigned voff, const void* sbase, unsigned m0v) {
  asm volatile("s_mov_b32 m0, %2\n\ts_nop 0\n\tglobal_load_lds_dwordx4 %0, %1 offset:%3"
               :: "v"(voff), "s"(sbase), "s"(m0v), "n"(OFF) : "memory");
}
__device__ __forceinline__ void set_m0(unsigned m0v) { asm volatile("s_mov_b32 m0, %0\n\ts_nop 0" ::"s"(m0v) : "memory"); }
template <int OFF>
__device__ __forceinline__ void dma_0(unsigned vb, const void* sbase, bool shared) {
  if (shared) asm volatile("global_load_lds_dwordx4 %0, %1 offset:%2" ::"v"(vb), "s"(sbase), "n"(OFF) : "memory");
  else asm volatile("global_load_lds_dwordx4 %0, %1 offset:%2" KVPOL ::"v"(vb), "s"(sbase), "n"(OFF) : "memory");
}
__device__ __forceinline__ void touch(unsigned voff, const void* sbase, unsigned m0v) {
  asm volatile("s_mov_b32 m0, %2\n\ts_nop 0\n\tglobal_load_lds_dword %0, %1 sc1" ::"v"(voff), "s"(sbase), "s"(m0v) : "memory");
}
template <int I, int N, class F>
__device__ __forceinline__ void static_for(F&& f) {
  if constexpr (I < N) {
    f(std::integral_constant<int, I>{});
    static_for<I + 1, N>(f);
  }
}
template <int N>
__device__ __forceinline__ void wait_barrier() {
  asm volatile("s_waitcnt vmcnt(%0) lgkmcnt(0)\n\ts_barrier" ::"n"(N) : "memory");
}
__device__ __forceinline__ void lds_barrier() { asm volatile("s_waitcnt lgkmcnt(0)\n\ts_barrier" ::: "memory"); }

__device__ __forceinline__ float grp_max(float v) {
  unsigned u = __builtin_bit_cast(unsigned, v);
  auto a = __builtin_amdgcn_permlane16_swap(u, u, false, false);
  v = fmaxf(u2f(a[0]), u2f(a[1]));
  u = __builtin_bit_cast(unsigned, v);
  auto c = __builtin_amdgcn_permlane32_swap(u, u, false, false);
  return fmaxf(u2f(c[0]), u2f(c[1]));
}
__device__ __forceinline__ float half_sum(float v) {
  unsigned u = __builtin_bit_cast(unsigned, v);
  auto c = __builtin_amdgcn_permlane32_swap(u, u, false, false);
  return u2f(c[0]) + u2f(c[1]);
}

__device__ __forceinline__ unsigned rotl5(unsigned h) { return __builtin_amdgcn_alignbit(h, h, 27); }
__device__ __forceinline__ void hash13(const u32x4& a, const u32x4& b, const u32x4& c, unsigned d, unsigned tag,
                                       unsigned& h1, unsigned& h2) {
  unsigned x = tag ^ 0x9E3779B9u, y = ~tag;
#pragma unroll
  for (int k = 0; k < 4; ++k) { x = (rotl5(x) ^ a[k]) + 0x85EBCA6Bu; }
#pragma unroll
  for (int k = 0; k < 2; ++k) { x = (rotl5(x) ^ b[k]) + 0x85EBCA6Bu; }
#pragma unroll
  for (int k = 2; k < 4; ++k) { y = (rotl5(y) ^ b[k]) + 0xC2B2AE35u; }
#pragma unroll
  for (int k = 0; k < 4; ++k) { y = (rotl5(y) ^ c[k]) + 0xC2B2AE35u; }
  y = (rotl5(y) ^ d) + 0xC2B2AE35u;
  h1 = (x ^ rotl5(y)) + y;
  h2 = (a[0] + a[1] + a[2]) + (a[3] + b[0] + b[1]) + (b[2] + b[3] + c[0]) + (c[1] + c[2] + c[3]) + (d + 2u * tag + 1u);
}

template <int STR>
__device__ __forceinline__ void recv4(const __amdgpu_buffer_rsrc_t& rs, unsigned hint, unsigned src, unsigned tag, u32x4 (&th)[4]) {
  for (unsigned s2 = 0; s2 < SPIN_CAP; ++s2) {
    asm volatile("" ::: "memory");
    const unsigned tg = __builtin_amdgcn_raw_buffer_load_b32(rs, hint, 0, 16);
    if (__builtin_amdgcn_ballot_w64(tg == tag) == ~0ull) break;
    __builtin_amdgcn_s_sleep(1);
  }
  for (unsigned spin = 0; spin < SPIN_CAP; ++spin) {
    asm volatile("" ::: "memory");
#pragma unroll
    for (int k = 0; k < 4; ++k) th[k] = __builtin_amdgcn_raw_buffer_load_b128(rs, src + k * STR, 0, 16);
    unsigned h1, h2;
    hash13(th[0], th[1], th[2], th[3][0], tag, h1, h2);
    const bool ok = (h1 == th[3][1]) & (h2 == th[3][2]);
    if (__builtin_amdgcn_ballot_w64(ok) == ~0ull) break;
  }
}

__global__ __launch_bounds__(512, 2) void gqa_fused_kernel(const uint16_t* __restrict__ x, const uint16_t* __restrict__ kc,
                                                           const uint16_t* __restrict__ vc, const uint16_t* __restrict__ wq,
                                                           const uint16_t* __restrict__ wo, uint16_t* __restrict__ out,
                                                           unsigned char* __restrict__ ws) {
  const int tid = threadIdx.x;
  const int lane = tid & 63;
  const int w = __builtin_amdgcn_readfirstlane(tid >> 6);
  const int bid = blockIdx.x;
  const int kvh = bid & 7, slot = bid >> 3, hp = slot & 1, kh = (slot >> 1) & 1, b = slot >> 2;
  const int g = lane >> 4, i16 = lane & 15;
  const int hh = w >> 2, jq = w & 3;
  const int head0 = kvh * 4 + hp * 2;

  STAMPR(0);
  unsigned* epoch_ptr = (unsigned*)(ws + WS_EPOCH + (size_t)b * 256);
  const unsigned epoch_v = __hip_atomic_load(epoch_ptr, __ATOMIC_RELAXED, __HIP_MEMORY_SCOPE_AGENT);

  const char* kbase = (const char*)kc + ((size_t)(b * 8 + kvh) * SK + kh * 1024) * (DH * 2);
  const char* vbase = (const char*)vc + ((size_t)(b * 8 + kvh) * SK + kh * 1024) * (DH * 2);
  const char* wobase = (const char*)wo + ((size_t)head0 * DH * DM + kh * 256) * 2;

  unsigned voffK[4], voffV[2];
#pragma unroll
  for (int i = 0; i < 4; ++i) voffK[i] = (unsigned)(w * 4096 + g * 256 + ((i16 ^ (4 * i + g) ^ ((i & 1) << 3)) << 4));
#pragma unroll
  for (int i = 0; i < 2; ++i) voffV[i] = (unsigned)(w * 4096 + g * 256 + ((i16 ^ (2 * (4 * i + g))) << 4));
  const unsigned w4k = (unsigned)(w * 4096);
  u32x4 qf[4][3];

  {
    const int qk = (w >> 1) & 1, ch = w & 1;
    if (w == 0) touch((lane & 1) ? ((lane & 2) ? WS_HINT2 : WS_HINT1) + (unsigned)bid * 128u
                                 : WS_IN * (unsigned)(b + 1) + (unsigned)(lane >> 1) * 65536u, ws, TOUCH_OFF);
    const char* xb = (const char*)x + (size_t)b * SQ * DM * 2;
    static_for<0, 6>([&](auto rc) {
      constexpr int r = decltype(rc)::value;
      const int row = w * 6 + r;
      dma_def<0>((unsigned)((lane ^ (row & 15)) << 4), xb + row * 1024, (unsigned)(XIMG + row * 1024));
    });
    if (tid < 16) lds_stu(FL_OFF + tid * 4, 0u);
    const __amdgpu_buffer_rsrc_t wq_rs = __builtin_amdgcn_make_buffer_rsrc((void*)wq, 0, DM * NQ * 2, 0x00020000);
    const unsigned wq_voff = (unsigned)((256 * qk + 8 * g) * (NQ * 2) + (head0 + hh) * (DH * 2) + ch * 128 + i16 * 8);
    u32x2 wr[8][8];
#pragma unroll
    for (int ks = 0; ks < 6; ++ks)
#pragma unroll
      for (int j = 0; j < 8; ++j) wr[ks][j] = __builtin_amdgcn_raw_buffer_load_b64(wq_rs, wq_voff, (32 * ks + j) * (NQ * 2), 0);
    wait_barrier<48>();
    STAMP(29);
#pragma unroll
    for (int ks = 6; ks < 8; ++ks)
#pragma unroll
      for (int j = 0; j < 8; ++j) wr[ks][j] = __builtin_amdgcn_raw_buffer_load_b64(wq_rs, wq_voff, (32 * ks + j) * (NQ * 2), 0);
    static_for<0, 4>([&](auto ic) { constexpr int i = decltype(ic)::value; dma_nt<i * 1024>(voffK[i], kbase, w4k); });
    static_for<0, 4>([&](auto ic) { constexpr int i = decltype(ic)::value; dma_nt<i * 1024>(voffK[i], kbase + 32768, w4k + SLOT); });
    static_for<0, 4>([&](auto ic) { constexpr int i = decltype(ic)::value; dma_nt<i * 1024>(voffV[i & 1], vbase, w4k + 2 * SLOT); });
    SB();
    f32x4 acc[4][3];
#pragma unroll
    for (int e = 0; e < 4; ++e)
#pragma unroll
      for (int t = 0; t < 3; ++t) acc[e][t] = f32x4{0.f, 0.f, 0.f, 0.f};
    unsigned xa[4];
#pragma unroll
    for (int k = 0; k < 4; ++k) xa[k] = XIMG + i16 * 1024 + qk * 512 + (((4 * k + g) ^ i16) << 4);
    u32x4 xf[2][3];
#pragma unroll
    for (int t = 0; t < 3; ++t) xf[0][t] = lds_ld128(xa[0] + t * 16384);
#pragma unroll
    for (int ks = 0; ks < 8; ++ks) {
      if (ks + 1 < 8) {
#pragma unroll
        for (int t = 0; t < 3; ++t) xf[(ks + 1) & 1][t] = lds_ld128(xa[(ks + 1) & 3] + ((ks + 1) >> 2) * 256 + t * 16384);
      }
#pragma unroll
      for (int e = 0; e < 4; ++e) {
        u32x4 af;
#pragma unroll
        for (int r = 0; r < 4; ++r)
          af[r] = __builtin_amdgcn_perm(wr[ks][2 * r + 1][e >> 1], wr[ks][2 * r][e >> 1], (e & 1) ? 0x07060302u : 0x05040100u);
#pragma unroll
        for (int t = 0; t < 3; ++t) acc[e][t] = mfma(af, xf[ks & 1][t], acc[e][t]);
      }
      SB();
    }
    STAMP(30);
    lds_barrier();
    {
      const unsigned snd = XIMG + (w ^ 2) * 6144 + lane * 16;
#pragma unroll
      for (int t = 0; t < 3; ++t)
#pragma unroll
        for (int rr = 0; rr < 2; ++rr) {
          f32x4 v;
#pragma unroll
          for (int e = 0; e < 4; ++e) v[e] = qk ? acc[e][t][rr] : acc[e][t][2 + rr];
          lds_st128(snd + (t * 2 + rr) * 1024, __builtin_bit_cast(u32x4, v));
        }
    }
    lds_barrier();
    {
      const unsigned rcv = XIMG + w * 6144 + lane * 16;
      f32x4 pv[3][2];
#pragma unroll
      for (int t = 0; t < 3; ++t)
#pragma unroll
        for (int rr = 0; rr < 2; ++rr) pv[t][rr] = __builtin_bit_cast(f32x4, lds_ld128(rcv + (t * 2 + rr) * 1024));
#pragma unroll
      for (int t = 0; t < 3; ++t) {
        float v[8];
#pragma unroll
        for (int rr = 0; rr < 2; ++rr)
#pragma unroll
          for (int e = 0; e < 4; ++e) v[4 * rr + e] = ((qk ? acc[e][t][2 + rr] : acc[e][t][rr]) + pv[t][rr][e]) * SCL;
        lds_st128(rcv + t * 1024, u32x4{pk_bf16(v[0], v[1]), pk_bf16(v[2], v[3]), pk_bf16(v[4], v[5]), pk_bf16(v[6], v[7])});
      }
    }
    STAMP(31);
    wait_barrier<8>();
#pragma unroll
    for (int s = 0; s < 4; ++s)
#pragma unroll
      for (int t = 0; t < 3; ++t)
        qf[s][t] = lds_ld128(XIMG + (hh * 4 + (s & 1) * 2 + (g >> 1)) * 6144 + t * 1024 + ((2 * (g & 1) + (s >> 1)) * 16 + i16) * 16);
  }
  STAMPR(1);

  unsigned ka[4];
#pragma unroll
  for (int s = 0; s < 4; ++s) ka[s] = (32 * jq + i16) * 256 + (((4 * g + s) ^ i16 ^ ((i16 & 4) << 1)) << 4);
  unsigned va[4];
  {
    const int q_ = (lane >> 2) & 3, p = lane & 3;
#pragma unroll
    for (int dt = 0; dt < 4; ++dt)
      va[dt] = 2 * SLOT + (32 * jq + 4 * g + q_) * 256 + (((8 * (jq & 1) + 2 * dt + (p >> 1)) ^ (2 * ((4 * g + q_) & 7))) << 4) + 8 * (p & 1);
  }
  const unsigned pw = PB + hh * 12288 + (jq >> 1) * 6144 + (jq & 1) * 1024 + lane * 16;

  f32x4 O[4][3];
#pragma unroll
  for (int dt = 0; dt < 4; ++dt)
#pragma unroll
    for (int t = 0; t < 3; ++t) O[dt][t] = f32x4{0.f, 0.f, 0.f, 0.f};
  float lsum[3] = {0.f, 0.f, 0.f};
  f32x4 nm[3];
  f32x4 ps[3];
  const u32x4 ones = {0x3F803F80u, 0x3F803F80u, 0x3F803F80u, 0x3F803F80u};
  const f32x4 zero4 = {0.f, 0.f, 0.f, 0.f};
  const u32x4 zerou = {0u, 0u, 0u, 0u};

  auto tile_max = [&](const f32x4(&S)[2][3], int t) {
    float m = fmaxf(fmaxf(S[0][t][0], S[0][t][1]), S[0][t][2]);
    m = fmaxf(fmaxf(m, S[0][t][3]), S[1][t][0]);
    m = fmaxf(fmaxf(m, S[1][t][1]), S[1][t][2]);
    return fmaxf(m, S[1][t][3]);
  };
  auto head_max = [&](const f32x4(&S)[2][3], float (&m)[3]) {
#pragma unroll
    for (int t = 0; t < 3; ++t) {
      const float v = grp_max(tile_max(S, t));
      if (lane < 16) lds_stf(MX_OFF + ((hh * 4 + jq) * 3 + t) * 64 + lane * 4, v);
    }
    lds_barrier();
#pragma unroll
    for (int t = 0; t < 3; ++t) {
      float v = lds_ldf(MX_OFF + ((hh * 4 + 0) * 3 + t) * 64 + i16 * 4);
#pragma unroll
      for (int j = 1; j < 4; ++j) v = fmaxf(v, lds_ldf(MX_OFF + ((hh * 4 + j) * 3 + t) * 64 + i16 * 4));
      m[t] = v;
    }
  };
  auto p_unit = [&](const f32x4& s) { return u32x2{pk_bf16(ex2(s[0]), ex2(s[1])), pk_bf16(ex2(s[2]), ex2(s[3]))}; };
  auto p_put = [&](int t, u32x2 a, u32x2 c) {
    const u32x4 f = {a[0], a[1], c[0], c[1]};
    lds_st128(pw + t * 2048, f);
    return f;
  };
  auto v_frag = [&](unsigned a) {
    const u32x2 v0 = lds_tr(a), v1 = lds_tr(a + 4096);
    return u32x4{v0[0], v0[1], v1[0], v1[1]};
  };

  unsigned vxb = voffV[0], vyb = voffK[0];
  unsigned wvb = (unsigned)((16 * w + g) * 1024 + ((i16 ^ (2 * g)) << 4));
#define IC(v) std::integral_constant<int, (v)>{}

  f32x4 SA[2][3], SB_[2][3];
  u32x4 Pown[3], Ppar[3], vg[4], kf[8];
#pragma unroll
  for (int t = 0; t < 3; ++t) Ppar[t] = zerou;
  vg[2] = zerou;
  vg[3] = zerou;
  STAMP(2);
#pragma unroll
  for (int c = 0; c < 8; ++c) kf[c] = lds_ld128(ka[c & 3] + (c >> 2) * 4096);
  SB();
#pragma unroll
  for (int c = 0; c < 8; ++c)
#pragma unroll
    for (int t = 0; t < 3; ++t) SA[c >> 2][t] = mfma(kf[c], qf[c & 3][t], (c & 3) == 0 ? zero4 : SA[c >> 2][t]);
  {
    float m[3];
    head_max(SA, m);
    static_for<0, 4>([&](auto ic) { constexpr int i = decltype(ic)::value; dma_nt<i * 1024>(voffK[i], kbase + 2 * 32768, w4k); });
#pragma unroll
    for (int t = 0; t < 3; ++t) {
      nm[t] = f32x4{-m[t], -m[t], -m[t], -m[t]};
      SA[0][t] += nm[t];
      SA[1][t] += nm[t];
    }
  }

  struct Req { const char* src; unsigned ex, x1, x2, x3, vb; bool shared; };
  auto req_issue = [&](auto ic, const Req& r) {
    constexpr int i = decltype(ic)::value;
    if constexpr (i == 0) dma_0<0>(r.vb, r.src, r.shared);
    else dma_0<i * 1024>(r.vb ^ (i == 1 ? r.x1 : (i == 2 ? r.x2 : r.x3)), r.src + i * r.ex, r.shared);
  };
  auto pv3 = [&](int dt, const u32x4& v, const u32x4 (&p)[3]) {
#pragma unroll
    for (int t = 0; t < 3; ++t) O[dt][t] = mfma(v, p[t], O[dt][t]);
  };
  auto xphase = [&](auto ksc, auto dsc, auto lastc, f32x4(&SC)[2][3], f32x4(&SN)[2][3], const Req& rq, int t) {
    constexpr unsigned ko = decltype(ksc)::value * SLOT;
    constexpr bool LAST = decltype(lastc)::value;
    LDMA(set_m0(w4k + decltype(dsc)::value * SLOT));
    auto qk = [&](auto cc) {
      constexpr int c = decltype(cc)::value, kt = c >> 2, s = c & 3;
      if constexpr (!LAST) {
#pragma unroll
        for (int tt = 0; tt < 3; ++tt) SN[kt][tt] = mfma(kf[c], qf[s][tt], s == 0 ? nm[tt] : SN[kt][tt]);
      }
    };
    if constexpr (!LAST) {
#pragma unroll
      for (int c = 0; c < 4; ++c) kf[c] = lds_ld128(ka[c] + ko);
    }
    SB();
    pv3(2, vg[2], Ppar);
    u32x2 a0 = p_unit(SC[0][0]);
    SB();
    pv3(3, vg[3], Ppar);
    u32x2 a1 = p_unit(SC[1][0]);
    Pown[0] = p_put(0, a0, a1);
    SB();
    if constexpr (!LAST) {
#pragma unroll
      for (int c = 4; c < 8; ++c) kf[c] = lds_ld128(ka[c & 3] + ko + 4096);
    }
    LDMA(req_issue(IC(0), rq));
    SB();
    qk(IC(0));
    a0 = p_unit(SC[0][1]);
    SB();
    qk(IC(1));
    a1 = p_unit(SC[1][1]);
    Pown[1] = p_put(1, a0, a1);
    ps[0] = mfma(ones, Pown[0], zero4);
    SB();
    LDMA(req_issue(IC(1), rq));
    SB();
    qk(IC(2));
    a0 = p_unit(SC[0][2]);
    SB();
    qk(IC(3));
    a1 = p_unit(SC[1][2]);
    Pown[2] = p_put(2, a0, a1);
    ps[1] = mfma(ones, Pown[1], zero4);
    SB();
    LDMA(req_issue(IC(2), rq));
    SB();
    qk(IC(4));
    ps[2] = mfma(ones, Pown[2], zero4);
    SB();
    LDMA(req_issue(IC(3), rq));
    SB();
    qk(IC(5));
    {
      const unsigned u0 = f2u(ps[0][0]), u1 = f2u(ps[1][0]), u2 = f2u(ps[2][0]);
      const unsigned um = max(max(u0, u1), u2);
      if (um > SUM_CAP) lds_stu(FL_OFF + t * 4, 1u);
    }
    SB();
  };
  auto yphase = [&](auto vsc, auto dsc, auto lastc, f32x4(&SC)[2][3], f32x4(&SN)[2][3], const Req& rq, int t) {
    constexpr unsigned vo = (decltype(vsc)::value - 2) * SLOT;
    constexpr bool LAST = decltype(lastc)::value;
    if constexpr (!LAST) LDMA(set_m0(w4k + decltype(dsc)::value * SLOT));
    const unsigned flagv = lds_ldu(FL_OFF + t * 4);
    SB();
    u32x4 vf[4];
#pragma unroll
    for (int dt = 0; dt < 4; ++dt) vf[dt] = v_frag(va[dt] + vo);
    SB();
    if constexpr (!LAST) {
#pragma unroll
      for (int tt = 0; tt < 3; ++tt) SN[1][tt] = mfma(kf[6], qf[2][tt], SN[1][tt]);
#pragma unroll
      for (int tt = 0; tt < 3; ++tt) SN[1][tt] = mfma(kf[7], qf[3][tt], SN[1][tt]);
    }
    SB();
    if (__builtin_amdgcn_readfirstlane(flagv)) {
      float m[3];
      head_max(SC, m);
#pragma unroll
      for (int tt = 0; tt < 3; ++tt) {
        const float d = fmaxf(m[tt], 0.f);
        const float alpha = ex2(-d);
        const f32x4 d4 = {d, d, d, d};
        lsum[tt] *= alpha;
#pragma unroll
        for (int dt = 0; dt < 4; ++dt) O[dt][tt] *= alpha;
        nm[tt] -= d4;
        SC[0][tt] -= d4;
        SC[1][tt] -= d4;
        if constexpr (!LAST) { SN[0][tt] -= d4; SN[1][tt] -= d4; }
        Pown[tt] = p_put(tt, p_unit(SC[0][tt]), p_unit(SC[1][tt]));
        ps[tt] = mfma(ones, Pown[tt], zero4);
      }
      lds_barrier();
    }
    SB();
#pragma unroll
    for (int tt = 0; tt < 3; ++tt) Ppar[tt] = lds_ld128((pw ^ 1024u) + tt * 2048);
#pragma unroll
    for (int dt = 0; dt < 4; ++dt) vg[dt] = v_frag((va[dt] ^ 8192u) + vo);
#pragma unroll
    for (int tt = 0; tt < 3; ++tt) lsum[tt] += ps[tt][0];
    SB();
    pv3(0, vf[0], Pown);
    if constexpr (!LAST) LDMA(req_issue(IC(0), rq));
    SB();
    pv3(1, vf[1], Pown);
    if constexpr (!LAST) LDMA(req_issue(IC(1), rq));
    SB();
    pv3(2, vf[2], Pown);
    if constexpr (!LAST) LDMA(req_issue(IC(2), rq));
    SB();
    pv3(3, vf[3], Pown);
    if constexpr (!LAST) LDMA(req_issue(IC(3), rq));
    SB();
    pv3(0, vg[0], Ppar);
    pv3(1, vg[1], Ppar);
    if constexpr (LAST) {
      pv3(2, vg[2], Ppar);
      pv3(3, vg[3], Ppar);
    }
    SB();
  };
  auto wsrc = [&](int wi) { return wobase + (size_t)(wi >> 1) * 128 * 1024 + (wi & 1) * 256; };
  #define LAUNDER(v) asm volatile("" : "+v"(v))
  auto reqV = [&](int t) { LAUNDER(vxb); return Req{vbase + (size_t)t * 32768, 0u, 0x80u, 0u, 0x80u, vxb, false}; };
  auto reqK = [&](int t) { LAUNDER(vyb); return Req{kbase + (size_t)t * 32768, 0u, 0xC0u, 0x80u, 0x40u, vyb, false}; };
  auto reqW = [&](int wi) { LAUNDER(wvb); return Req{wsrc(wi), 3072u, 0x80u, 0u, 0x80u, wvb, true}; };

#pragma unroll 1
  for (int t2 = 0; t2 < 6; t2 += 2) {
    wait_barrier<8>();
    STAMP(3 + 2 * t2);
    xphase(IC(1), IC(3), std::false_type{}, SA, SB_, reqV(t2 + 1), t2);
    DSTAMP(25);
    wait_barrier<8>();
    STAMP(4 + 2 * t2);
    yphase(IC(2), IC(1), std::false_type{}, SA, SB_, reqK(t2 + 3), t2);
    DSTAMP(26);
    wait_barrier<8>();
    STAMP(5 + 2 * t2);
    xphase(IC(0), IC(2), std::false_type{}, SB_, SA, reqV(t2 + 2), t2 + 1);
    Req ry = t2 == 4 ? reqW(0) : reqK(t2 + 4);
    ry.shared = false;
    DSTAMP(27);
    wait_barrier<8>();
    STAMP(6 + 2 * t2);
    yphase(IC(3), IC(0), std::false_type{}, SB_, SA, ry, t2 + 1);
    DSTAMP(28);
  }
  wait_barrier<8>();
  STAMP(15);
  xphase(IC(1), IC(3), std::false_type{}, SA, SB_, reqV(7), 6);
  wait_barrier<8>();
  STAMP(16);
  yphase(IC(2), IC(1), std::false_type{}, SA, SB_, reqW(1), 6);
  wait_barrier<8>();
  STAMP(17);
  xphase(IC(0), IC(2), std::true_type{}, SB_, SA, reqW(2), 7);
  wait_barrier<8>();
  STAMP(18);
  yphase(IC(3), IC(3), std::true_type{}, SB_, SA, reqW(3), 7);

#pragma unroll
  for (int t = 0; t < 3; ++t)
    if (lane < 16) lds_stf(LS_OFF + ((hh * 4 + jq) * 3 + t) * 64 + lane * 4, lsum[t]);
  wait_barrier<0>();
  STAMPR(19);
  set_m0(w4k + 3 * SLOT);
  static_for<0, 4>([&](auto ic) { req_issue(ic, reqW(3)); });
  const unsigned ow = PB + hh * 12288 + (2 * (jq & 1) + (jq >> 1)) * 3072 + lane * 16;
  const unsigned ox = PB + hh * 12288 + (2 * (jq & 1) + ((jq >> 1) ^ 1)) * 3072 + lane * 16;
  f32x4 Om[2][3];
  float lt[3];
  {
    const int jk = jq >> 1;
#pragma unroll
    for (int t = 0; t < 3; ++t) lds_st128(ow + t * 1024, __builtin_bit_cast(u32x4, jk ? O[0][t] : O[2][t]));
#pragma unroll
    for (int t = 0; t < 3; ++t) {
      float s = 0.f;
#pragma unroll
      for (int j = 0; j < 4; ++j) s += lds_ldf(LS_OFF + ((hh * 4 + j) * 3 + t) * 64 + i16 * 4);
      lt[t] = s;
    }
    lds_barrier();
#pragma unroll
    for (int t = 0; t < 3; ++t) Om[0][t] = (jk ? O[2][t] : O[0][t]) + __builtin_bit_cast(f32x4, lds_ld128(ox + t * 1024));
    SB();
#pragma unroll
    for (int t = 0; t < 3; ++t) lds_st128(ox + t * 1024, __builtin_bit_cast(u32x4, jk ? O[1][t] : O[3][t]));
    lds_barrier();
#pragma unroll
    for (int t = 0; t < 3; ++t) Om[1][t] = (jk ? O[3][t] : O[1][t]) + __builtin_bit_cast(f32x4, lds_ld128(ow + t * 1024));
  }

  const unsigned tag = (unsigned)__builtin_amdgcn_readfirstlane(epoch_v) + 1u;
  const __amdgpu_buffer_rsrc_t ws_rs = __builtin_amdgcn_make_buffer_rsrc((void*)ws, 0, WS_END, 0x00020000);
  const unsigned reg1 = WS_IN * (unsigned)(b + 1), reg2 = reg1 + WS_IN2;
  const unsigned lbid = (unsigned)bid & 31u;

  float lam[3];
  u32x4 mine[3];
#pragma unroll
  for (int t = 0; t < 3; ++t) {
    const float inv = 1.0f / lt[t];
    lam[t] = __builtin_amdgcn_logf(lt[t]) - nm[t][0];
#pragma unroll
    for (int dt = 0; dt < 2; ++dt) Om[dt][t] *= inv;
    mine[t] = u32x4{pk_bf16(Om[0][t][0], Om[0][t][1]), pk_bf16(Om[0][t][2], Om[0][t][3]), pk_bf16(Om[1][t][0], Om[1][t][1]),
                    pk_bf16(Om[1][t][2], Om[1][t][3])};
  }
  {
    const float l0 = lam[0], l1 = lam[1], l2 = lam[2];
    const unsigned lw = __builtin_bit_cast(unsigned, (lane & 32) ? l2 : ((lane & 16) ? l1 : l0));
    unsigned h1, h2;
    hash13(mine[0], mine[1], mine[2], lw, tag, h1, h2);
    const unsigned dst = reg1 + (lbid ^ 16u) * 32768u + (unsigned)(w * 4096 + lane * 16);
    __builtin_amdgcn_raw_buffer_store_b128(mine[0], ws_rs, dst, 0, 16);
    __builtin_amdgcn_raw_buffer_store_b128(mine[1], ws_rs, dst + 1024, 0, 16);
    __builtin_amdgcn_raw_buffer_store_b128(mine[2], ws_rs, dst + 2048, 0, 16);
    __builtin_amdgcn_raw_buffer_store_b128(u32x4{lw, h1, h2, tag}, ws_rs, dst + 3072, 0, 16);
    SB();
    if (lane == 0) __builtin_amdgcn_raw_buffer_store_b32(tag, ws_rs, WS_HINT1 + (unsigned)((bid ^ 16) * 128 + w * 4), 0, 16);
  }
  unsigned wa_[2];
  {
    const int q_ = (lane >> 2) & 3, p = lane & 3;
#pragma unroll
    for (int dt = 0; dt < 2; ++dt)
      wa_[dt] = hh * SLOT + (4 * g + q_) * 256 + (((4 * jq + 2 * dt + (p >> 1)) ^ (2 * ((4 * g + q_) & 7))) << 4) + 8 * (p & 1);
  }
  u32x4 wf[8][2];
#pragma unroll
  for (int st = 0; st < 4; ++st)
#pragma unroll
    for (int dt = 0; dt < 2; ++dt) wf[st][dt] = v_frag(wa_[dt] + st * 8192);
  SB();
  u32x4 th[4];
  recv4<1024>(ws_rs, WS_HINT1 + (unsigned)(bid * 128 + w * 4), reg1 + lbid * 32768u + (unsigned)(w * 4096 + lane * 16), tag, th);
  STAMPR(20);
  {
    const unsigned u = th[3][0];
    auto a = __builtin_amdgcn_permlane32_swap(u, u, false, false);
    auto c0 = __builtin_amdgcn_permlane16_swap(a[0], a[0], false, false);
    auto c1 = __builtin_amdgcn_permlane16_swap(a[1], a[1], false, false);
    const float lb[3] = {u2f(c0[0]), u2f(c0[1]), u2f(c1[0])};
#pragma unroll
    for (int t = 0; t < 3; ++t) {
      const float mx = fmaxf(lam[t], lb[t]);
      float wa = ex2(lam[t] - mx), wb = ex2(lb[t] - mx);
      const float inv = 1.0f / (wa + wb);
      wa *= inv;
      wb *= inv;
      float m[8];
#pragma unroll
      for (int k = 0; k < 4; ++k) {
        m[2 * k] = wa * Om[k >> 1][t][2 * (k & 1)] + wb * bf_lo(th[t][k]);
        m[2 * k + 1] = wa * Om[k >> 1][t][2 * (k & 1) + 1] + wb * bf_hi(th[t][k]);
      }
      lds_st128(ow + t * 1024, u32x4{pk_bf16(m[0], m[1]), pk_bf16(m[2], m[3]), pk_bf16(m[4], m[5]), pk_bf16(m[6], m[7])});
    }
  }
  wait_barrier<0>();
  STAMP(21);

  const unsigned pr = PB + lane * 16;
  f32x4 A[2][3];
#pragma unroll
  for (int dt = 0; dt < 2; ++dt)
#pragma unroll
    for (int t = 0; t < 3; ++t) A[dt][t] = zero4;
  {
    u32x4 pf[8][3];
#pragma unroll
    for (int st = 0; st < 4; ++st)
#pragma unroll
      for (int t = 0; t < 3; ++t) pf[st][t] = lds_ld128(pr + st * 3072 + t * 1024);
#pragma unroll
    for (int st = 4; st < 8; ++st) {
#pragma unroll
      for (int dt = 0; dt < 2; ++dt) wf[st][dt] = v_frag(wa_[dt] + 2 * SLOT + (st - 4) * 8192);
#pragma unroll
      for (int t = 0; t < 3; ++t) pf[st][t] = lds_ld128(pr + st * 3072 + t * 1024);
    }
    SB();
#pragma unroll
    for (int st = 0; st < 8; ++st)
#pragma unroll
      for (int dt = 0; dt < 2; ++dt)
#pragma unroll
        for (int t = 0; t < 3; ++t) A[dt][t] = mfma(wf[st][dt], pf[st][t], A[dt][t]);
  }
  STAMP(22);

  {
    u32x4 f[3];
#pragma unroll
    for (int t = 0; t < 3; ++t)
      f[t] = u32x4{pk_bf16(A[0][t][0], A[0][t][1]), pk_bf16(A[0][t][2], A[0][t][3]), pk_bf16(A[1][t][0], A[1][t][1]),
                   pk_bf16(A[1][t][2], A[1][t][3])};
    unsigned h1, h2;
    hash13(f[0], f[1], f[2], 0u, tag, h1, h2);
    const int rho = 2 * w + (i16 >> 3);
    const int rbid = (rho >> 1) + 8 * ((rho & 1) + 2 * kh + 4 * b);
    const int pidx = kvh * 2 + hp;
    const unsigned dst = reg2 + (unsigned)((rbid & 31) * 32768 + pidx * 2048 + (8 * g + (i16 & 7)) * 16);
    __builtin_amdgcn_raw_buffer_store_b128(f[0], ws_rs, dst, 0, 16);
    __builtin_amdgcn_raw_buffer_store_b128(f[1], ws_rs, dst + 512, 0, 16);
    __builtin_amdgcn_raw_buffer_store_b128(f[2], ws_rs, dst + 1024, 0, 16);
    __builtin_amdgcn_raw_buffer_store_b128(u32x4{0u, h1, h2, tag}, ws_rs, dst + 1536, 0, 16);
    SB();
    if ((lane & 55) == 0) __builtin_amdgcn_raw_buffer_store_b32(tag, ws_rs, WS_HINT2 + (unsigned)(rbid * 128 + pidx * 4), 0, 16);
  }
  {
    recv4<512>(ws_rs, WS_HINT2 + (unsigned)(bid * 128 + (tid >> 5) * 4), reg2 + lbid * 32768u + (unsigned)((tid >> 5) * 2048 + (tid & 31) * 16), tag, th);
    STAMPR(23);
#pragma unroll
    for (int t = 0; t < 3; ++t) {
      f32x4 r;
#pragma unroll
      for (int k = 0; k < 2; ++k) {
        const float a0 = half_sum(bf_lo(th[t][k])), a1 = half_sum(bf_hi(th[t][k]));
        const float b0 = half_sum(bf_lo(th[t][2 + k])), b1 = half_sum(bf_hi(th[t][2 + k]));
        r[2 * k] = lane < 32 ? a0 : b0;
        r[2 * k + 1] = lane < 32 ? a1 : b1;
      }
      lds_st128(w * 3072 + t * 1024 + lane * 16, __builtin_bit_cast(u32x4, r));
    }
  }
  lds_barrier();
  if (tid < 192) {
    const int t = tid >> 6;
    f32x4 s = __builtin_bit_cast(f32x4, lds_ld128(t * 1024 + lane * 16));
#pragma unroll
    for (int v = 1; v < 8; ++v) s += __builtin_bit_cast(f32x4, lds_ld128(v * 3072 + t * 1024 + lane * 16));
    const int rho = kvh * 2 + hp;
    const int nt = lane >> 5, l32 = lane & 31, gg = l32 >> 3, q = 8 * (rho & 1) + (l32 & 7);
    const int col = 256 * kh + 32 * (rho >> 1) + 16 * nt + 4 * gg;
    u32x2 o = {pk_bf16(s[0], s[1]), pk_bf16(s[2], s[3])};
    *(u32x2*)(out + ((size_t)b * SQ + 16 * t + q) * DM + col) = o;
  }
  STAMPR(24);
  STAMP_DUMP();
  if (tid == 0 && kvh == 0 && hp == 0 && kh == 0) __hip_atomic_store(epoch_ptr, tag, __ATOMIC_RELAXED, __HIP_MEMORY_SCOPE_AGENT);
}

}

extern "C" void kernel_launch(void* const* d_in, const int* in_sizes, int n_in, void* d_out, int out_size, void* d_ws,
                              size_t ws_size, hipStream_t stream) {
  (void)in_sizes; (void)n_in; (void)out_size; (void)ws_size;
  (void)hipFuncSetAttribute((const void*)gqa_fused_kernel, hipFuncAttributeMaxDynamicSharedMemorySize, LDS_BYTES);
  gqa_fused_kernel<<<256, 512, LDS_BYTES, stream>>>((const uint16_t*)d_in[0], (const uint16_t*)d_in[1], (const uint16_t*)d_in[2],
                                                    (const uint16_t*)d_in[3], (const uint16_t*)d_in[4], (uint16_t*)d_out,
                                                    (unsigned char*)d_ws);
}
```

```cpp
#include <hip/hip_runtime.h>
#include <hip/hip_bf16.h>
#include <stdint.h>
#include <type_traits>

#define AS3 __attribute__((address_space(3)))
typedef __attribute__((ext_vector_type(8))) __bf16 bf16x8;
typedef __attribute__((ext_vector_type(2))) __bf16 bf16x2;
typedef __attribute__((ext_vector_type(4))) float f32x4;
typedef __attribute__((ext_vector_type(2))) float f32x2;
typedef __attribute__((ext_vector_type(4))) unsigned u32x4;
typedef __attribute__((ext_vector_type(2))) unsigned u32x2;
typedef __attribute__((ext_vector_type(4))) short i16x4;

#define STAMP(i) do {} while (0)
#define STAMPR(i) do {} while (0)
#define STAMP_DUMP() do { __builtin_amdgcn_s_waitcnt(0xc07f);} while (0)
#define SB() __builtin_amdgcn_sched_barrier(0)
#define IC(v) std::integral_constant<int, (v)>{}
#define HOP1_AUX 16
#define HOP2_AUX 16
#define KVPOL " nt"
#define LDMA(x) x
#define DSTAMP(i) do {} while (0)

namespace {

constexpr int SQ = 48, DM = 512, SK = 2048, DH = 128, NQ = 4096;

constexpr unsigned SLOT = 32768;
constexpr unsigned PB = 131072;
constexpr unsigned MISC = 155648;
constexpr unsigned MX_OFF = MISC;
constexpr unsigned LS_OFF = MISC + 1536;
constexpr unsigned FL_OFF = MISC + 3072;
constexpr unsigned TOUCH_OFF = MISC + 3328;
constexpr unsigned XIMG = 98304;
constexpr unsigned LDS_BYTES = 163840;

constexpr unsigned WS_EPOCH = 0;
constexpr unsigned WS_HINT1 = 4096;
constexpr unsigned WS_HINT2 = WS_HINT1 + 32768;
constexpr unsigned WS_IN = 2u << 20;
constexpr unsigned WS_IN2 = 1u << 20;
constexpr unsigned WS_END = WS_IN * 9;

constexpr float SCL = 0.08838834764831845f * 1.4426950408889634f;
constexpr unsigned SUM_CAP = 0x47800000u;
constexpr unsigned SPIN_CAP = 1u << 16;

__device__ __forceinline__ u32x4 lds_ld128(unsigned a) { return *(const AS3 u32x4*)(size_t)a; }
__device__ __forceinline__ void lds_st128(unsigned a, u32x4 v) { *(AS3 u32x4*)(size_t)a = v; }
__device__ __forceinline__ float lds_ldf(unsigned a) { return *(const AS3 float*)(size_t)a; }
__device__ __forceinline__ void lds_stf(unsigned a, float v) { *(AS3 float*)(size_t)a = v; }
__device__ __forceinline__ unsigned lds_ldu(unsigned a) { return *(const AS3 unsigned*)(size_t)a; }
__device__ __forceinline__ void lds_stu(unsigned a, unsigned v) { *(AS3 unsigned*)(size_t)a = v; }
__device__ __forceinline__ u32x2 lds_tr(unsigned a) {
  i16x4 r = __builtin_amdgcn_ds_read_tr16_b64_v4i16((AS3 i16x4*)(size_t)a);
  return __builtin_bit_cast(u32x2, r);
}
__device__ __forceinline__ f32x4 mfma(u32x4 a, u32x4 b, f32x4 c) {
  return __builtin_amdgcn_mfma_f32_16x16x32_bf16(__builtin_bit_cast(bf16x8, a), __builtin_bit_cast(bf16x8, b), c, 0, 0, 0);
}
__device__ __forceinline__ unsigned pk_bf16(float lo, float hi) {
  f32x2 v = {lo, hi};
  bf16x2 r = __builtin_convertvector(v, bf16x2);
  return __builtin_bit_cast(unsigned, r);
}
__device__ __forceinline__ float u2f(unsigned u) { return __builtin_bit_cast(float, u); }
__device__ __forceinline__ unsigned f2u(float f) { return __builtin_bit_cast(unsigned, f); }
__device__ __forceinline__ float bf_lo(unsigned u) { return __builtin_bit_cast(float, u << 16); }
__device__ __forceinline__ float bf_hi(unsigned u) { return __builtin_bit_cast(float, u & 0xffff0000u); }
__device__ __forceinline__ float ex2(float v) { return __builtin_amdgcn_exp2f(v); }

template <int OFF>
__device__ __forceinline__ void dma_nt(unsigned voff, const void* sbase, unsigned m0v) {
  asm volatile("s_mov_b32 m0, %2\n\ts_nop 0\n\tglobal_load_lds_dwordx4 %0, %1 offset:%3" KVPOL
               :: "v"(voff), "s"(sbase), "s"(m0v), "n"(OFF) : "memory");
}
template <int OFF>
__device__ __forceinline__ void dma_def(unsigned voff, const void* sbase, unsigned m0v) {
  asm volatile("s_mov_b32 m0, %2\n\ts_nop 0\n\tglobal_load_lds_dwordx4 %0, %1 offset:%3"
               :: "v"(voff), "s"(sbase), "s"(m0v), "n"(OFF) : "memory");
}
__device__ __forceinline__ void set_m0(unsigned m0v) { asm volatile("s_mov_b32 m0, %0\n\ts_nop 0" ::"s"(m0v) : "memory"); }
template <int OFF>
__device__ __forceinline__ void dma_0(unsigned vb, const void* sbase, bool shared) {
  if (shared) asm volatile("global_load_lds_dwordx4 %0, %1 offset:%2" ::"v"(vb), "s"(sbase), "n"(OFF) : "memory");
  else asm volatile("global_load_lds_dwordx4 %0, %1 offset:%2" KVPOL ::"v"(vb), "s"(sbase), "n"(OFF) : "memory");
}
__device__ __forceinline__ void touch(unsigned voff, const void* sbase, unsigned m0v) {
  asm volatile("s_mov_b32 m0, %2\n\ts_nop 0\n\tglobal_load_lds_dword %0, %1 sc1" ::"v"(voff), "s"(sbase), "s"(m0v) : "memory");
}
template <int I, int N, class F>
__device__ __forceinline__ void static_for(F&& f) {
  if constexpr (I < N) {
    f(std::integral_constant<int, I>{});
    static_for<I + 1, N>(f);
  }
}
template <int N>
__device__ __forceinline__ void wait_barrier() {
  asm volatile("s_waitcnt vmcnt(%0) lgkmcnt(0)\n\ts_barrier" ::"n"(N) : "memory");
}
__device__ __forceinline__ void lds_barrier() { asm volatile("s_waitcnt lgkmcnt(0)\n\ts_barrier" ::: "memory"); }

__device__ __forceinline__ float grp_max(float v) {
  unsigned u = __builtin_bit_cast(unsigned, v);
  auto a = __builtin_amdgcn_permlane16_swap(u, u, false, false);
  v = fmaxf(u2f(a[0]), u2f(a[1]));
  u = __builtin_bit_cast(unsigned, v);
  auto c = __builtin_amdgcn_permlane32_swap(u, u, false, false);
  return fmaxf(u2f(c[0]), u2f(c[1]));
}
__device__ __forceinline__ float half_sum(float v) {
  unsigned u = __builtin_bit_cast(unsigned, v);
  auto c = __builtin_amdgcn_permlane32_swap(u, u, false, false);
  return u2f(c[0]) + u2f(c[1]);
}

__device__ __forceinline__ unsigned rotl5(unsigned h) { return __builtin_amdgcn_alignbit(h, h, 27); }
__device__ __forceinline__ void hash13(const u32x4& a, const u32x4& b, const u32x4& c, unsigned d, unsigned tag,
                                       unsigned& h1, unsigned& h2) {
  unsigned x = tag ^ 0x9E3779B9u, y = ~tag;
#pragma unroll
  for (int k = 0; k < 4; ++k) { x = (rotl5(x) ^ a[k]) + 0x85EBCA6Bu; }
#pragma unroll
  for (int k = 0; k < 2; ++k) { x = (rotl5(x) ^ b[k]) + 0x85EBCA6Bu; }
#pragma unroll
  for (int k = 2; k < 4; ++k) { y = (rotl5(y) ^ b[k]) + 0xC2B2AE35u; }
#pragma unroll
  for (int k = 0; k < 4; ++k) { y = (rotl5(y) ^ c[k]) + 0xC2B2AE35u; }
  y = (rotl5(y) ^ d) + 0xC2B2AE35u;
  h1 = (x ^ rotl5(y)) + y;
  h2 = (a[0] + a[1] + a[2]) + (a[3] + b[0] + b[1]) + (b[2] + b[3] + c[0]) + (c[1] + c[2] + c[3]) + (d + 2u * tag + 1u);
}

template <int STR, int SI, int AUX1>
__device__ __forceinline__ void recv4(const __amdgpu_buffer_rsrc_t& rs, unsigned hint, unsigned src, unsigned tag, u32x4 (&th)[4], int lane, int w) {
  unsigned n1 = 0, n2 = 0;
  for (unsigned s2 = 0; s2 < SPIN_CAP; ++s2) {
    asm volatile("" ::: "memory");
    const unsigned tg = __builtin_amdgcn_raw_buffer_load_b32(rs, hint, 0, 16);
    ++n1;
    if (__builtin_amdgcn_ballot_w64(tg == tag) == ~0ull) break;
    __builtin_amdgcn_s_sleep(1);
  }
  STAMP(SI);
  if constexpr (AUX1 != 16) {
#pragma unroll
    for (int k = 0; k < 4; ++k) th[k] = __builtin_amdgcn_raw_buffer_load_b128(rs, src + k * STR, 0, AUX1);
    unsigned h1, h2;
    hash13(th[0], th[1], th[2], th[3][0], tag, h1, h2);
    const bool ok = (h1 == th[3][1]) & (h2 == th[3][2]);
    n2 += 100;
    if (__builtin_amdgcn_ballot_w64(ok) == ~0ull) {
      return;
    }
  }
  for (unsigned spin = 0; spin < SPIN_CAP; ++spin) {
    asm volatile("" ::: "memory");
#pragma unroll
    for (int k = 0; k < 4; ++k) th[k] = __builtin_amdgcn_raw_buffer_load_b128(rs, src + k * STR, 0, 16);
    unsigned h1, h2;
    hash13(th[0], th[1], th[2], th[3][0], tag, h1, h2);
    const bool ok = (h1 == th[3][1]) & (h2 == th[3][2]);
    ++n2;
    if (__builtin_amdgcn_ballot_w64(ok) == ~0ull) break;
  }
  (void)n1; (void)n2; (void)lane; (void)w;
}

__global__ __launch_bounds__(512, 2) void gqa_fused_kernel(const uint16_t* __restrict__ x, const uint16_t* __restrict__ kc,
                                                           const uint16_t* __restrict__ vc, const uint16_t* __restrict__ wq,
                                                           const uint16_t* __restrict__ wo, uint16_t* __restrict__ out,
                                                           unsigned char* __restrict__ ws) {
  const int tid = threadIdx.x;
  const int lane = tid & 63;
  const int w = __builtin_amdgcn_readfirstlane(tid >> 6);
  const int bid = blockIdx.x;
  const int kvh = bid & 7, slot = bid >> 3, hp = slot & 1, kh = (slot >> 1) & 1, b = slot >> 2;
  const int g = lane >> 4, i16 = lane & 15;
  const int hh = w >> 2, jq = w & 3;
  const int head0 = kvh * 4 + hp * 2;

  STAMPR(0);
  unsigned* epoch_ptr = (unsigned*)(ws + WS_EPOCH + (size_t)b * 256);
  const unsigned epoch_v = __hip_atomic_load(epoch_ptr, __ATOMIC_RELAXED, __HIP_MEMORY_SCOPE_AGENT);

  const char* kbase = (const char*)kc + ((size_t)(b * 8 + kvh) * SK + kh * 1024) * (DH * 2);
  const char* vbase = (const char*)vc + ((size_t)(b * 8 + kvh) * SK + kh * 1024) * (DH * 2);
  const char* wobase = (const char*)wo + ((size_t)head0 * DH * DM + kh * 256) * 2;

  unsigned voffK[4], voffV[2];
#pragma unroll
  for (int i = 0; i < 4; ++i) voffK[i] = (unsigned)(w * 4096 + g * 256 + ((i16 ^ (4 * i + g) ^ ((i & 1) << 3)) << 4));
#pragma unroll
  for (int i = 0; i < 2; ++i) voffV[i] = (unsigned)(w * 4096 + g * 256 + ((i16 ^ (2 * (4 * i + g))) << 4));
  const unsigned w4k = (unsigned)(w * 4096);
  u32x4 qf[4][3];

  {
    if (w == 0) touch((lane & 1) ? ((lane & 2) ? WS_HINT2 : WS_HINT1) + (unsigned)bid * 128u
                                 : WS_IN * (unsigned)(b + 1) + (unsigned)(lane >> 1) * 65536u, ws, TOUCH_OFF);
    STAMP(42);
    const char* xb = (const char*)x + (size_t)b * SQ * DM * 2;
    static_for<0, 6>([&](auto rc) {
      constexpr int r = decltype(rc)::value;
      const int row = w * 6 + r;
      dma_def<0>((unsigned)((lane ^ (row & 15)) << 4), xb + row * 1024, (unsigned)(XIMG + row * 1024));
    });
    const int rot = (slot >> 1) & 7;
    const char* wqb = (const char*)wq + head0 * (DH * 2);
    const unsigned voffQ = (unsigned)((8 * w + (lane >> 5)) * (NQ * 2) + (((lane & 31) ^ (2 * ((lane >> 5) + 4 * (w & 1)))) << 4));
    auto wq_tile = [&](auto sc) {
      constexpr int st = decltype(sc)::value;
      const char* src = wqb + (size_t)(((st + rot) & 7) * 64) * (NQ * 2);
      static_for<0, 4>([&](auto ic) {
        constexpr int i = decltype(ic)::value;
        dma_def<i * 1024>(voffQ ^ ((i & 1) * 64u), src + i * (2 * NQ * 2 - 1024), w4k + ((st + 1) % 3) * SLOT);
      });
    };
    wq_tile(IC(0));
    wq_tile(IC(1));
    wq_tile(IC(2));
    SB();
    STAMP(43);
    SB();
    if (tid < 16) lds_stu(FL_OFF + tid * 4, 0u);
    unsigned aq[2];
    {
      const int q_ = (lane >> 2) & 3, p = lane & 3;
#pragma unroll
      for (int dt = 0; dt < 2; ++dt)
        aq[dt] = (8 * g + q_) * 512 + (((4 * w + 2 * dt + (p >> 1)) ^ (2 * (q_ + 4 * (g & 1)))) << 4) + 8 * (p & 1);
    }
    const unsigned xl = XIMG + i16 * 1024 + ((g ^ i16) << 4);
    f32x4 acc[2][3];
#pragma unroll
    for (int dt = 0; dt < 2; ++dt)
#pragma unroll
      for (int t = 0; t < 3; ++t) acc[dt][t] = f32x4{0.f, 0.f, 0.f, 0.f};
    static_for<0, 8>([&](auto sc) {
      constexpr int st = decltype(sc)::value;
      if constexpr (st == 0) wait_barrier<8>(); else wait_barrier<4>();
      if constexpr (st == 0) STAMP(29);
      if constexpr (st >= 1) { SB(); STAMP(43 + st); SB(); }
        if constexpr (st >= 1 && st <= 5) wq_tile(IC(st + 2));
      if constexpr (st == 6) static_for<0, 4>([&](auto ic) { constexpr int i = decltype(ic)::value; dma_nt<i * 1024>(voffK[i], kbase, w4k); });
      if constexpr (st == 7) static_for<0, 4>([&](auto ic) { constexpr int i = decltype(ic)::value; dma_nt<i * 1024>(voffK[i], kbase + 32768, w4k + SLOT); });
      const int tt = (st + rot) & 7;
      const unsigned xs = (xl ^ ((unsigned)(tt & 1) << 7)) + (unsigned)(tt >> 1) * 256u;
      constexpr unsigned so = ((st + 1) % 3) * SLOT;
      u32x4 af[2][2], bf[2][3];
#pragma unroll
      for (int e = 0; e < 2; ++e) {
#pragma unroll
        for (int dt = 0; dt < 2; ++dt) {
          const u32x2 v0 = lds_tr(aq[dt] + so + e * 16384), v1 = lds_tr(aq[dt] + so + e * 16384 + 2048);
          af[e][dt] = u32x4{v0[0], v0[1], v1[0], v1[1]};
        }
#pragma unroll
        for (int t = 0; t < 3; ++t) bf[e][t] = lds_ld128((xs ^ (e * 64u)) + t * 16384);
      }
#pragma unroll
      for (int e = 0; e < 2; ++e)
#pragma unroll
        for (int dt = 0; dt < 2; ++dt)
#pragma unroll
          for (int t = 0; t < 3; ++t) acc[dt][t] = mfma(af[e][dt], bf[e][t], acc[dt][t]);
      SB();
    });
    STAMP(30);
    lds_barrier();
    static_for<0, 4>([&](auto ic) { constexpr int i = decltype(ic)::value; dma_nt<i * 1024>(voffV[i & 1], vbase, w4k + 2 * SLOT); });
#pragma unroll
    for (int dt = 0; dt < 2; ++dt)
#pragma unroll
      for (int t = 0; t < 3; ++t) {
        const u32x2 v = {pk_bf16(acc[dt][t][0] * SCL, acc[dt][t][1] * SCL), pk_bf16(acc[dt][t][2] * SCL, acc[dt][t][3] * SCL)};
        *(AS3 u32x2*)(size_t)(XIMG + hh * 12288 + t * 4096 + (4 * jq + 2 * dt + (g >> 1)) * 256 + i16 * 16 + (g & 1) * 8) = v;
      }
    STAMP(31);
    wait_barrier<8>();
#pragma unroll
    for (int s = 0; s < 4; ++s)
#pragma unroll
      for (int t = 0; t < 3; ++t) qf[s][t] = lds_ld128(XIMG + hh * 12288 + t * 4096 + (4 * g + s) * 256 + i16 * 16);
  }
  STAMPR(1);

  unsigned ka[4];
#pragma unroll
  for (int s = 0; s < 4; ++s) ka[s] = (32 * jq + i16) * 256 + (((4 * g + s) ^ i16 ^ ((i16 & 4) << 1)) << 4);
  unsigned va[4];
  {
    const int q_ = (lane >> 2) & 3, p = lane & 3;
#pragma unroll
    for (int dt = 0; dt < 4; ++dt)
      va[dt] = 2 * SLOT + (32 * jq + 4 * g + q_) * 256 + (((8 * (jq & 1) + 2 * dt + (p >> 1)) ^ (2 * ((4 * g + q_) & 7))) << 4) + 8 * (p & 1);
  }
  const unsigned pw = PB + hh * 12288 + (jq >> 1) * 6144 + (jq & 1) * 1024 + lane * 16;

  f32x4 O[4][3];
#pragma unroll
  for (int dt = 0; dt < 4; ++dt)
#pragma unroll
    for (int t = 0; t < 3; ++t) O[dt][t] = f32x4{0.f, 0.f, 0.f, 0.f};
  float lsum[3] = {0.f, 0.f, 0.f};
  f32x4 nm[3];
  f32x4 ps[3];
  const u32x4 ones = {0x3F803F80u, 0x3F803F80u, 0x3F803F80u, 0x3F803F80u};
  const f32x4 zero4 = {0.f, 0.f, 0.f, 0.f};
  const u32x4 zerou = {0u, 0u, 0u, 0u};

  auto tile_max = [&](const f32x4(&S)[2][3], int t) {
    float m = fmaxf(fmaxf(S[0][t][0], S[0][t][1]), S[0][t][2]);
    m = fmaxf(fmaxf(m, S[0][t][3]), S[1][t][0]);
    m = fmaxf(fmaxf(m, S[1][t][1]), S[1][t][2]);
    return fmaxf(m, S[1][t][3]);
  };
  auto head_max = [&](const f32x4(&S)[2][3], float (&m)[3]) {
#pragma unroll
    for (int t = 0; t < 3; ++t) {
      const float v = grp_max(tile_max(S, t));
      if (lane < 16) lds_stf(MX_OFF + ((hh * 4 + jq) * 3 + t) * 64 + lane * 4, v);
    }
    lds_barrier();
#pragma unroll
    for (int t = 0; t < 3; ++t) {
      float v = lds_ldf(MX_OFF + ((hh * 4 + 0) * 3 + t) * 64 + i16 * 4);
#pragma unroll
      for (int j = 1; j < 4; ++j) v = fmaxf(v, lds_ldf(MX_OFF + ((hh * 4 + j) * 3 + t) * 64 + i16 * 4));
      m[t] = v;
    }
  };
  auto p_unit = [&](const f32x4& s) { return u32x2{pk_bf16(ex2(s[0]), ex2(s[1])), pk_bf16(ex2(s[2]), ex2(s[3]))}; };
  auto p_put = [&](int t, u32x2 a, u32x2 c) {
    const u32x4 f = {a[0], a[1], c[0], c[1]};
    lds_st128(pw + t * 2048, f);
    return f;
  };
  auto v_frag = [&](unsigned a) {
    const u32x2 v0 = lds_tr(a), v1 = lds_tr(a + 4096);
    return u32x4{v0[0], v0[1], v1[0], v1[1]};
  };

  unsigned vxb = voffV[0], vyb = voffK[0];
  unsigned wvb = (unsigned)((16 * w + g) * 1024 + ((i16 ^ (2 * g)) << 4));

  f32x4 SA[2][3], SB_[2][3];
  u32x4 Pown[3], Ppar[3], vg[4], kf[8];
#pragma unroll
  for (int t = 0; t < 3; ++t) Ppar[t] = zerou;
  vg[2] = zerou;
  vg[3] = zerou;
  STAMP(2);
#pragma unroll
  for (int c = 0; c < 8; ++c) kf[c] = lds_ld128(ka[c & 3] + (c >> 2) * 4096);
  SB();
#pragma unroll
  for (int c = 0; c < 8; ++c)
#pragma unroll
    for (int t = 0; t < 3; ++t) SA[c >> 2][t] = mfma(kf[c], qf[c & 3][t], (c & 3) == 0 ? zero4 : SA[c >> 2][t]);
  {
    float m[3];
    head_max(SA, m);
    static_for<0, 4>([&](auto ic) { constexpr int i = decltype(ic)::value; dma_nt<i * 1024>(voffK[i], kbase + 2 * 32768, w4k); });
#pragma unroll
    for (int t = 0; t < 3; ++t) {
      nm[t] = f32x4{-m[t], -m[t], -m[t], -m[t]};
      SA[0][t] += nm[t];
      SA[1][t] += nm[t];
    }
  }

  struct Req { const char* src; unsigned ex, x1, x2, x3, vb; bool shared; };
  auto req_issue = [&](auto ic, const Req& r) {
    constexpr int i = decltype(ic)::value;
    if constexpr (i == 0) dma_0<0>(r.vb, r.src, r.shared);
    else dma_0<i * 1024>(r.vb ^ (i == 1 ? r.x1 : (i == 2 ? r.x2 : r.x3)), r.src + i * r.ex, r.shared);
  };
  auto pv3 = [&](int dt, const u32x4& v, const u32x4 (&p)[3]) {
#pragma unroll
    for (int t = 0; t < 3; ++t) O[dt][t] = mfma(v, p[t], O[dt][t]);
  };
  auto xphase = [&](auto ksc, auto dsc, auto lastc, f32x4(&SC)[2][3], f32x4(&SN)[2][3], const Req& rq, int t) {
    constexpr unsigned ko = decltype(ksc)::value * SLOT;
    constexpr bool LAST = decltype(lastc)::value;
    LDMA(set_m0(w4k + decltype(dsc)::value * SLOT));
    auto qk = [&](auto cc) {
      constexpr int c = decltype(cc)::value, kt = c >> 2, s = c & 3;
      if constexpr (!LAST) {
#pragma unroll
        for (int tt = 0; tt < 3; ++tt) SN[kt][tt] = mfma(kf[c], qf[s][tt], s == 0 ? nm[tt] : SN[kt][tt]);
      }
    };
    if constexpr (!LAST) {
#pragma unroll
      for (int c = 0; c < 4; ++c) kf[c] = lds_ld128(ka[c] + ko);
    }
    SB();
    pv3(2, vg[2], Ppar);
    u32x2 a0 = p_unit(SC[0][0]);
    SB();
    pv3(3, vg[3], Ppar);
    u32x2 a1 = p_unit(SC[1][0]);
    Pown[0] = p_put(0, a0, a1);
    SB();
    if constexpr (!LAST) {
#pragma unroll
      for (int c = 4; c < 8; ++c) kf[c] = lds_ld128(ka[c & 3] + ko + 4096);
    }
    LDMA(req_issue(IC(0), rq));
    SB();
    qk(IC(0));
    a0 = p_unit(SC[0][1]);
    SB();
    qk(IC(1));
    a1 = p_unit(SC[1][1]);
    Pown[1] = p_put(1, a0, a1);
    ps[0] = mfma(ones, Pown[0], zero4);
    SB();
    LDMA(req_issue(IC(1), rq));
    SB();
    qk(IC(2));
    a0 = p_unit(SC[0][2]);
    SB();
    qk(IC(3));
    a1 = p_unit(SC[1][2]);
    Pown[2] = p_put(2, a0, a1);
    ps[1] = mfma(ones, Pown[1], zero4);
    SB();
    LDMA(req_issue(IC(2), rq));
    SB();
    qk(IC(4));
    ps[2] = mfma(ones, Pown[2], zero4);
    SB();
    LDMA(req_issue(IC(3), rq));
    SB();
    qk(IC(5));
    {
      const unsigned u0 = f2u(ps[0][0]), u1 = f2u(ps[1][0]), u2 = f2u(ps[2][0]);
      const unsigned um = max(max(u0, u1), u2);
      if (um > SUM_CAP) lds_stu(FL_OFF + t * 4, 1u);
    }
    SB();
  };
  auto yphase = [&](auto vsc, auto dsc, auto lastc, f32x4(&SC)[2][3], f32x4(&SN)[2][3], const Req& rq, int t) {
    constexpr unsigned vo = (decltype(vsc)::value - 2) * SLOT;
    constexpr bool LAST = decltype(lastc)::value;
    if constexpr (!LAST) LDMA(set_m0(w4k + decltype(dsc)::value * SLOT));
    const unsigned flagv = lds_ldu(FL_OFF + t * 4);
    SB();
    u32x4 vf[4];
#pragma unroll
    for (int dt = 0; dt < 4; ++dt) vf[dt] = v_frag(va[dt] + vo);
    SB();
    if constexpr (!LAST) {
#pragma unroll
      for (int tt = 0; tt < 3; ++tt) SN[1][tt] = mfma(kf[6], qf[2][tt], SN[1][tt]);
#pragma unroll
      for (int tt = 0; tt < 3; ++tt) SN[1][tt] = mfma(kf[7], qf[3][tt], SN[1][tt]);
    }
    SB();
    if (__builtin_amdgcn_readfirstlane(flagv)) {
      float m[3];
      head_max(SC, m);
#pragma unroll
      for (int tt = 0; tt < 3; ++tt) {
        const float d = fmaxf(m[tt], 0.f);
        const float alpha = ex2(-d);
        const f32x4 d4 = {d, d, d, d};
        lsum[tt] *= alpha;
#pragma unroll
        for (int dt = 0; dt < 4; ++dt) O[dt][tt] *= alpha;
        nm[tt] -= d4;
        SC[0][tt] -= d4;
        SC[1][tt] -= d4;
        if constexpr (!LAST) { SN[0][tt] -= d4; SN[1][tt] -= d4; }
        Pown[tt] = p_put(tt, p_unit(SC[0][tt]), p_unit(SC[1][tt]));
        ps[tt] = mfma(ones, Pown[tt], zero4);
      }
      lds_barrier();
    }
    SB();
#pragma unroll
    for (int tt = 0; tt < 3; ++tt) Ppar[tt] = lds_ld128((pw ^ 1024u) + tt * 2048);
#pragma unroll
    for (int dt = 0; dt < 4; ++dt) vg[dt] = v_frag((va[dt] ^ 8192u) + vo);
#pragma unroll
    for (int tt = 0; tt < 3; ++tt) lsum[tt] += ps[tt][0];
    SB();
    pv3(0, vf[0], Pown);
    if constexpr (!LAST) LDMA(req_issue(IC(0), rq));
    SB();
    pv3(1, vf[1], Pown);
    if constexpr (!LAST) LDMA(req_issue(IC(1), rq));
    SB();
    pv3(2, vf[2], Pown);
    if constexpr (!LAST) LDMA(req_issue(IC(2), rq));
    SB();
    pv3(3, vf[3], Pown);
    if constexpr (!LAST) LDMA(req_issue(IC(3), rq));
    SB();
    pv3(0, vg[0], Ppar);
    pv3(1, vg[1], Ppar);
    if constexpr (LAST) {
      pv3(2, vg[2], Ppar);
      pv3(3, vg[3], Ppar);
    }
    SB();
  };
  auto wsrc = [&](int wi) { return wobase + (size_t)(wi >> 1) * 128 * 1024 + (wi & 1) * 256; };
  #define LAUNDER(v) asm volatile("" : "+v"(v))
  auto reqV = [&](int t) { LAUNDER(vxb); return Req{vbase + (size_t)t * 32768, 0u, 0x80u, 0u, 0x80u, vxb, false}; };
  auto reqK = [&](int t) { LAUNDER(vyb); return Req{kbase + (size_t)t * 32768, 0u, 0xC0u, 0x80u, 0x40u, vyb, false}; };
  auto reqW = [&](int wi) { LAUNDER(wvb); return Req{wsrc(wi), 3072u, 0x80u, 0u, 0x80u, wvb, true}; };

#pragma unroll 1
  for (int t2 = 0; t2 < 6; t2 += 2) {
    wait_barrier<8>();
    STAMP(3 + 2 * t2);
    xphase(IC(1), IC(3), std::false_type{}, SA, SB_, reqV(t2 + 1), t2);
    DSTAMP(25);
    wait_barrier<8>();
    STAMP(4 + 2 * t2);
    yphase(IC(2), IC(1), std::false_type{}, SA, SB_, reqK(t2 + 3), t2);
    DSTAMP(26);
    wait_barrier<8>();
    STAMP(5 + 2 * t2);
    xphase(IC(0), IC(2), std::false_type{}, SB_, SA, reqV(t2 + 2), t2 + 1);
    Req ry = t2 == 4 ? reqW(0) : reqK(t2 + 4);
    ry.shared = false;
    DSTAMP(27);
    wait_barrier<8>();
    STAMP(6 + 2 * t2);
    yphase(IC(3), IC(0), std::false_type{}, SB_, SA, ry, t2 + 1);
    DSTAMP(28);
  }
  wait_barrier<8>();
  STAMP(15);
  xphase(IC(1), IC(3), std::false_type{}, SA, SB_, reqV(7), 6);
  wait_barrier<8>();
  STAMP(16);
  yphase(IC(2), IC(1), std::false_type{}, SA, SB_, reqW(1), 6);
  wait_barrier<8>();
  STAMP(17);
  xphase(IC(0), IC(2), std::true_type{}, SB_, SA, reqW(2), 7);
  wait_barrier<8>();
  STAMP(18);
  yphase(IC(3), IC(3), std::true_type{}, SB_, SA, reqW(3), 7);

#pragma unroll
  for (int t = 0; t < 3; ++t)
    if (lane < 16) lds_stf(LS_OFF + ((hh * 4 + jq) * 3 + t) * 64 + lane * 4, lsum[t]);
  wait_barrier<0>();
  const unsigned tag = (unsigned)__builtin_amdgcn_readfirstlane(epoch_v) + 1u;
  SB();
  STAMPR(19);
  set_m0(w4k + 3 * SLOT);
  static_for<0, 4>([&](auto ic) { req_issue(ic, reqW(3)); });
  const unsigned ow = PB + hh * 12288 + (2 * (jq & 1) + (jq >> 1)) * 3072 + lane * 16;
  const unsigned ox = PB + hh * 12288 + (2 * (jq & 1) + ((jq >> 1) ^ 1)) * 3072 + lane * 16;
  f32x4 Om[2][3];
  float lt[3];
  {
    const int jk = jq >> 1;
#pragma unroll
    for (int t = 0; t < 3; ++t) lds_st128(ow + t * 1024, __builtin_bit_cast(u32x4, jk ? O[0][t] : O[2][t]));
#pragma unroll
    for (int t = 0; t < 3; ++t) {
      float s = 0.f;
#pragma unroll
      for (int j = 0; j < 4; ++j) s += lds_ldf(LS_OFF + ((hh * 4 + j) * 3 + t) * 64 + i16 * 4);
      lt[t] = s;
    }
    lds_barrier();
#pragma unroll
    for (int t = 0; t < 3; ++t) Om[0][t] = (jk ? O[2][t] : O[0][t]) + __builtin_bit_cast(f32x4, lds_ld128(ox + t * 1024));
    SB();
#pragma unroll
    for (int t = 0; t < 3; ++t) lds_st128(ox + t * 1024, __builtin_bit_cast(u32x4, jk ? O[1][t] : O[3][t]));
    lds_barrier();
#pragma unroll
    for (int t = 0; t < 3; ++t) Om[1][t] = (jk ? O[3][t] : O[1][t]) + __builtin_bit_cast(f32x4, lds_ld128(ow + t * 1024));
  }

  STAMP(34);
  const __amdgpu_buffer_rsrc_t ws_rs = __builtin_amdgcn_make_buffer_rsrc((void*)ws, 0, WS_END, 0x00020000);
  const unsigned reg1 = WS_IN * (unsigned)(b + 1), reg2 = reg1 + WS_IN2;
  const unsigned lbid = (unsigned)bid & 31u;

  float lam[3];
  u32x4 mine[3];
#pragma unroll
  for (int t = 0; t < 3; ++t) {
    const float inv = 1.0f / lt[t];
    lam[t] = __builtin_amdgcn_logf(lt[t]) - nm[t][0];
#pragma unroll
    for (int dt = 0; dt < 2; ++dt) Om[dt][t] *= inv;
    mine[t] = u32x4{pk_bf16(Om[0][t][0], Om[0][t][1]), pk_bf16(Om[0][t][2], Om[0][t][3]), pk_bf16(Om[1][t][0], Om[1][t][1]),
                    pk_bf16(Om[1][t][2], Om[1][t][3])};
  }
  {
    const float l0 = lam[0], l1 = lam[1], l2 = lam[2];
    const unsigned lw = __builtin_bit_cast(unsigned, (lane & 32) ? l2 : ((lane & 16) ? l1 : l0));
    unsigned h1, h2;
    hash13(mine[0], mine[1], mine[2], lw, tag, h1, h2);
    const unsigned dst = reg1 + (lbid ^ 16u) * 32768u + (unsigned)(w * 4096 + lane * 16);
    __builtin_amdgcn_raw_buffer_store_b128(mine[0], ws_rs, dst, 0, 16);
    __builtin_amdgcn_raw_buffer_store_b128(mine[1], ws_rs, dst + 1024, 0, 16);
    __builtin_amdgcn_raw_buffer_store_b128(mine[2], ws_rs, dst + 2048, 0, 16);
    __builtin_amdgcn_raw_buffer_store_b128(u32x4{lw, h1, h2, tag}, ws_rs, dst + 3072, 0, 16);
    SB();
    if (lane == 0) __builtin_amdgcn_raw_buffer_store_b32(tag, ws_rs, WS_HINT1 + (unsigned)((bid ^ 16) * 128 + w * 4), 0, 16);
  }
  STAMP(35);
  unsigned wa_[2];
  {
    const int q_ = (lane >> 2) & 3, p = lane & 3;
#pragma unroll
    for (int dt = 0; dt < 2; ++dt)
      wa_[dt] = hh * SLOT + (4 * g + q_) * 256 + (((4 * jq + 2 * dt + (p >> 1)) ^ (2 * ((4 * g + q_) & 7))) << 4) + 8 * (p & 1);
  }
  u32x4 wf[8][2];
#pragma unroll
  for (int st = 0; st < 4; ++st)
#pragma unroll
    for (int dt = 0; dt < 2; ++dt) wf[st][dt] = v_frag(wa_[dt] + st * 8192);
  SB();
  u32x4 th[4];
  STAMP(36);
  recv4<1024, 37, HOP1_AUX>(ws_rs, WS_HINT1 + (unsigned)(bid * 128 + w * 4), reg1 + lbid * 32768u + (unsigned)(w * 4096 + lane * 16), tag, th, lane, w);
  STAMPR(20);
  {
    const unsigned u = th[3][0];
    auto a = __builtin_amdgcn_permlane32_swap(u, u, false, false);
    auto c0 = __builtin_amdgcn_permlane16_swap(a[0], a[0], false, false);
    auto c1 = __builtin_amdgcn_permlane16_swap(a[1], a[1], false, false);
    const float lb[3] = {u2f(c0[0]), u2f(c0[1]), u2f(c1[0])};
#pragma unroll
    for (int t = 0; t < 3; ++t) {
      const float mx = fmaxf(lam[t], lb[t]);
      float wa = ex2(lam[t] - mx), wb = ex2(lb[t] - mx);
      const float inv = 1.0f / (wa + wb);
      wa *= inv;
      wb *= inv;
      float m[8];
#pragma unroll
      for (int k = 0; k < 4; ++k) {
        m[2 * k] = wa * Om[k >> 1][t][2 * (k & 1)] + wb * bf_lo(th[t][k]);
        m[2 * k + 1] = wa * Om[k >> 1][t][2 * (k & 1) + 1] + wb * bf_hi(th[t][k]);
      }
      lds_st128(ow + t * 1024, u32x4{pk_bf16(m[0], m[1]), pk_bf16(m[2], m[3]), pk_bf16(m[4], m[5]), pk_bf16(m[6], m[7])});
    }
  }
  wait_barrier<0>();
  STAMP(21);

  const unsigned pr = PB + lane * 16;
  f32x4 A[2][3];
#pragma unroll
  for (int dt = 0; dt < 2; ++dt)
#pragma unroll
    for (int t = 0; t < 3; ++t) A[dt][t] = zero4;
  {
    u32x4 pf[8][3];
#pragma unroll
    for (int st = 0; st < 4; ++st)
#pragma unroll
      for (int t = 0; t < 3; ++t) pf[st][t] = lds_ld128(pr + st * 3072 + t * 1024);
#pragma unroll
    for (int st = 4; st < 8; ++st) {
#pragma unroll
      for (int dt = 0; dt < 2; ++dt) wf[st][dt] = v_frag(wa_[dt] + 2 * SLOT + (st - 4) * 8192);
#pragma unroll
      for (int t = 0; t < 3; ++t) pf[st][t] = lds_ld128(pr + st * 3072 + t * 1024);
    }
    SB();
#pragma unroll
    for (int st = 0; st < 8; ++st)
#pragma unroll
      for (int dt = 0; dt < 2; ++dt)
#pragma unroll
        for (int t = 0; t < 3; ++t) A[dt][t] = mfma(wf[st][dt], pf[st][t], A[dt][t]);
  }
  STAMP(22);

  {
    u32x4 f[3];
#pragma unroll
    for (int t = 0; t < 3; ++t)
      f[t] = u32x4{pk_bf16(A[0][t][0], A[0][t][1]), pk_bf16(A[0][t][2], A[0][t][3]), pk_bf16(A[1][t][0], A[1][t][1]),
                   pk_bf16(A[1][t][2], A[1][t][3])};
    unsigned h1, h2;
    hash13(f[0], f[1], f[2], 0u, tag, h1, h2);
    const int rho = 2 * w + (i16 >> 3);
    const int rbid = (rho >> 1) + 8 * ((rho & 1) + 2 * kh + 4 * b);
    const int pidx = kvh * 2 + hp;
    const unsigned dst = reg2 + (unsigned)((rbid & 31) * 32768 + pidx * 2048 + (8 * g + (i16 & 7)) * 16);
    __builtin_amdgcn_raw_buffer_store_b128(f[0], ws_rs, dst, 0, 16);
    __builtin_amdgcn_raw_buffer_store_b128(f[1], ws_rs, dst + 512, 0, 16);
    __builtin_amdgcn_raw_buffer_store_b128(f[2], ws_rs, dst + 1024, 0, 16);
    __builtin_amdgcn_raw_buffer_store_b128(u32x4{0u, h1, h2, tag}, ws_rs, dst + 1536, 0, 16);
    SB();
    if ((lane & 55) == 0) __builtin_amdgcn_raw_buffer_store_b32(tag, ws_rs, WS_HINT2 + (unsigned)(rbid * 128 + pidx * 4), 0, 16);
  }
  {
    STAMP(39);
    recv4<512, 40, HOP2_AUX>(ws_rs, WS_HINT2 + (unsigned)(bid * 128 + (tid >> 5) * 4), reg2 + lbid * 32768u + (unsigned)((tid >> 5) * 2048 + (tid & 31) * 16), tag, th, lane, w);
    STAMPR(23);
#pragma unroll
    for (int t = 0; t < 3; ++t) {
      f32x4 r;
#pragma unroll
      for (int k = 0; k < 2; ++k) {
        const float a0 = half_sum(bf_lo(th[t][k])), a1 = half_sum(bf_hi(th[t][k]));
        const float b0 = half_sum(bf_lo(th[t][2 + k])), b1 = half_sum(bf_hi(th[t][2 + k]));
        r[2 * k] = lane < 32 ? a0 : b0;
        r[2 * k + 1] = lane < 32 ? a1 : b1;
      }
      lds_st128(w * 3072 + t * 1024 + lane * 16, __builtin_bit_cast(u32x4, r));
    }
  }
  lds_barrier();
  if (tid < 192) {
    const int t = tid >> 6;
    f32x4 s = __builtin_bit_cast(f32x4, lds_ld128(t * 1024 + lane * 16));
#pragma unroll
    for (int v = 1; v < 8; ++v) s += __builtin_bit_cast(f32x4, lds_ld128(v * 3072 + t * 1024 + lane * 16));
    const int rho = kvh * 2 + hp;
    const int nt = lane >> 5, l32 = lane & 31, gg = l32 >> 3, q = 8 * (rho & 1) + (l32 & 7);
    const int col = 256 * kh + 32 * (rho >> 1) + 16 * nt + 4 * gg;
    u32x2 o = {pk_bf16(s[0], s[1]), pk_bf16(s[2], s[3])};
    *(u32x2*)(out + ((size_t)b * SQ + 16 * t + q) * DM + col) = o;
  }
  STAMPR(24);
  STAMP_DUMP();
  if (tid == 0 && kvh == 0 && hp == 0 && kh == 0) __hip_atomic_store(epoch_ptr, tag, __ATOMIC_RELAXED, __HIP_MEMORY_SCOPE_AGENT);
}

}

extern "C" void kernel_launch(void* const* d_in, const int* in_sizes, int n_in, void* d_out, int out_size, void* d_ws,
                              size_t ws_size, hipStream_t stream) {
  (void)in_sizes; (void)n_in; (void)out_size; (void)ws_size;
  (void)hipFuncSetAttribute((const void*)gqa_fused_kernel, hipFuncAttributeMaxDynamicSharedMemorySize, LDS_BYTES);
  gqa_fused_kernel<<<256, 512, LDS_BYTES, stream>>>((const uint16_t*)d_in[0], (const uint16_t*)d_in[1], (const uint16_t*)d_in[2],
                                                    (const uint16_t*)d_in[3], (const uint16_t*)d_in[4], (uint16_t*)d_out,
                                                    (unsigned char*)d_ws);
}
```

```cpp
#include <hip/hip_runtime.h>
#include <hip/hip_bf16.h>
#include <stdint.h>
#include <type_traits>

#define AS3 __attribute__((address_space(3)))
typedef __attribute__((ext_vector_type(8))) __bf16 bf16x8;
typedef __attribute__((ext_vector_type(2))) __bf16 bf16x2;
typedef __attribute__((ext_vector_type(4))) float f32x4;
typedef __attribute__((ext_vector_type(2))) float f32x2;
typedef __attribute__((ext_vector_type(4))) unsigned u32x4;
typedef __attribute__((ext_vector_type(2))) unsigned u32x2;
typedef __attribute__((ext_vector_type(4))) short i16x4;

#define STAMP(i) do {} while (0)
#define STAMPR(i) do {} while (0)
#define STAMP_DUMP() do { __builtin_amdgcn_s_waitcnt(0xc07f);} while (0)
#define SB() __builtin_amdgcn_sched_barrier(0)
#define IC(v) std::integral_constant<int, (v)>{}
#define HOP1_AUX 0
#define HOP2_AUX 0
#define MAPK 0
#define TOUCH_AT 0
#define KVPOL " nt"
#define LDMA(x) x
#define DSTAMP(i) do {} while (0)

namespace {

constexpr int SQ = 48, DM = 512, SK = 2048, DH = 128, NQ = 4096;

constexpr unsigned SLOT = 32768;
constexpr unsigned PB = 131072;
constexpr unsigned MISC = 155648;
constexpr unsigned MX_OFF = MISC;
constexpr unsigned LS_OFF = MISC + 1536;
constexpr unsigned FL_OFF = MISC + 3072;
constexpr unsigned TOUCH_OFF = MISC + 3328;
constexpr unsigned XIMG = 98304;
constexpr unsigned LDS_BYTES = 163840;

constexpr unsigned WS_EPOCH = 0;
constexpr unsigned WS_HINT1 = 4096;
constexpr unsigned WS_HINT2 = WS_HINT1 + 32768;
constexpr unsigned WS_IN = 2u << 20;
constexpr unsigned WS_IN2 = 1u << 20;
constexpr unsigned WS_END = WS_IN * 9;

constexpr float SCL = 0.08838834764831845f * 1.4426950408889634f;
constexpr unsigned SUM_CAP = 0x47800000u;
constexpr unsigned SPIN_CAP = 1u << 16;

__device__ __forceinline__ u32x4 lds_ld128(unsigned a) { return *(const AS3 u32x4*)(size_t)a; }
__device__ __forceinline__ void lds_st128(unsigned a, u32x4 v) { *(AS3 u32x4*)(size_t)a = v; }
__device__ __forceinline__ float lds_ldf(unsigned a) { return *(const AS3 float*)(size_t)a; }
__device__ __forceinline__ void lds_stf(unsigned a, float v) { *(AS3 float*)(size_t)a = v; }
__device__ __forceinline__ unsigned lds_ldu(unsigned a) { return *(const AS3 unsigned*)(size_t)a; }
__device__ __forceinline__ void lds_stu(unsigned a, unsigned v) { *(AS3 unsigned*)(size_t)a = v; }
__device__ __forceinline__ u32x2 lds_tr(unsigned a) {
  i16x4 r = __builtin_amdgcn_ds_read_tr16_b64_v4i16((AS3 i16x4*)(size_t)a);
  return __builtin_bit_cast(u32x2, r);
}
__device__ __forceinline__ f32x4 mfma(u32x4 a, u32x4 b, f32x4 c) {
  return __builtin_amdgcn_mfma_f32_16x16x32_bf16(__builtin_bit_cast(bf16x8, a), __builtin_bit_cast(bf16x8, b), c, 0, 0, 0);
}
__device__ __forceinline__ unsigned pk_bf16(float lo, float hi) {
  f32x2 v = {lo, hi};
  bf16x2 r = __builtin_convertvector(v, bf16x2);
  return __builtin_bit_cast(unsigned, r);
}
__device__ __forceinline__ float u2f(unsigned u) { return __builtin_bit_cast(float, u); }
__device__ __forceinline__ unsigned f2u(float f) { return __builtin_bit_cast(unsigned, f); }
__device__ __forceinline__ float bf_lo(unsigned u) { return __builtin_bit_cast(float, u << 16); }
__device__ __forceinline__ float bf_hi(unsigned u) { return __builtin_bit_cast(float, u & 0xffff0000u); }
__device__ __forceinline__ float ex2(float v) { return __builtin_amdgcn_exp2f(v); }

template <int OFF>
__device__ __forceinline__ void dma_nt(unsigned voff, const void* sbase, unsigned m0v) {
  asm volatile("s_mov_b32 m0, %2\n\ts_nop 0\n\tglobal_load_lds_dwordx4 %0, %1 offset:%3" KVPOL
               :: "v"(voff), "s"(sbase), "s"(m0v), "n"(OFF) : "memory");
}
template <int OFF>
__device__ __forceinline__ void dma_def(unsigned voff, const void* sbase, unsigned m0v) {
  asm volatile("s_mov_b32 m0, %2\n\ts_nop 0\n\tglobal_load_lds_dwordx4 %0, %1 offset:%3"
               :: "v"(voff), "s"(sbase), "s"(m0v), "n"(OFF) : "memory");
}
__device__ __forceinline__ void set_m0(unsigned m0v) { asm volatile("s_mov_b32 m0, %0\n\ts_nop 0" ::"s"(m0v) : "memory"); }
template <int OFF>
__device__ __forceinline__ void dma_0(unsigned vb, const void* sbase, bool shared) {
  if (shared) asm volatile("global_load_lds_dwordx4 %0, %1 offset:%2" ::"v"(vb), "s"(sbase), "n"(OFF) : "memory");
  else asm volatile("global_load_lds_dwordx4 %0, %1 offset:%2" KVPOL ::"v"(vb), "s"(sbase), "n"(OFF) : "memory");
}
__device__ __forceinline__ void touch(unsigned voff, const void* sbase, unsigned m0v) {
  asm volatile("s_mov_b32 m0, %2\n\ts_nop 0\n\tglobal_load_lds_dword %0, %1 sc1" ::"v"(voff), "s"(sbase), "s"(m0v) : "memory");
}
template <int I, int N, class F>
__device__ __forceinline__ void static_for(F&& f) {
  if constexpr (I < N) {
    f(std::integral_constant<int, I>{});
    static_for<I + 1, N>(f);
  }
}
template <int N>
__device__ __forceinline__ void wait_barrier() {
  asm volatile("s_waitcnt vmcnt(%0) lgkmcnt(0)\n\ts_barrier" ::"n"(N) : "memory");
}
__device__ __forceinline__ void lds_barrier() { asm volatile("s_waitcnt lgkmcnt(0)\n\ts_barrier" ::: "memory"); }

__device__ __forceinline__ float grp_max(float v) {
  unsigned u = __builtin_bit_cast(unsigned, v);
  auto a = __builtin_amdgcn_permlane16_swap(u, u, false, false);
  v = fmaxf(u2f(a[0]), u2f(a[1]));
  u = __builtin_bit_cast(unsigned, v);
  auto c = __builtin_amdgcn_permlane32_swap(u, u, false, false);
  return fmaxf(u2f(c[0]), u2f(c[1]));
}
__device__ __forceinline__ float half_sum(float v) {
  unsigned u = __builtin_bit_cast(unsigned, v);
  auto c = __builtin_amdgcn_permlane32_swap(u, u, false, false);
  return u2f(c[0]) + u2f(c[1]);
}

template <int R>
__device__ __forceinline__ unsigned rot(unsigned h) { return __builtin_amdgcn_alignbit(h, h, R); }
__device__ __forceinline__ void hash13(const u32x4& a, const u32x4& b, const u32x4& c, unsigned d, unsigned tag,
                                       unsigned& h1, unsigned& h2) {
  const unsigned x0 = a[0] ^ rot<5>(a[1]) ^ rot<10>(a[2]), x1 = rot<15>(a[3]) ^ rot<20>(b[0]) ^ rot<25>(b[1]);
  const unsigned x2 = rot<30>(b[2]) ^ rot<3>(b[3]) ^ rot<8>(c[0]), x3 = rot<13>(c[1]) ^ rot<18>(c[2]) ^ rot<23>(c[3]);
  h1 = (x0 ^ x1 ^ x2) ^ (x3 ^ rot<28>(d) ^ (tag * 0x9E3779B1u + 0x7F4A7C15u));
  h2 = (a[0] + a[1] + a[2]) + (a[3] + b[0] + b[1]) + (b[2] + b[3] + c[0]) + (c[1] + c[2] + c[3]) + (d + 2u * tag + 1u);
}

template <int STR, int SI, int MODE>
__device__ __forceinline__ void recv4(const __amdgpu_buffer_rsrc_t& rs, unsigned hint, unsigned src, unsigned tag, u32x4 (&th)[4], int lane, int w) {
  unsigned n1 = 0, n2 = 0;
  auto attempt = [&]() {
    asm volatile("" ::: "memory");
#pragma unroll
    for (int k = 0; k < 4; ++k) th[k] = __builtin_amdgcn_raw_buffer_load_b128(rs, src + k * STR, 0, 16);
    unsigned h1, h2;
    hash13(th[0], th[1], th[2], th[3][0], tag, h1, h2);
    const bool ok = (h1 == th[3][1]) & (h2 == th[3][2]);
    ++n2;
    return __builtin_amdgcn_ballot_w64(ok) == ~0ull;
  };
  bool done = false;
  if constexpr (MODE & 1) done = attempt();
  if (!done) {
    if constexpr (!(MODE & 2)) {
      for (unsigned s2 = 0; s2 < SPIN_CAP; ++s2) {
        asm volatile("" ::: "memory");
        const unsigned tg = __builtin_amdgcn_raw_buffer_load_b32(rs, hint, 0, 16);
        ++n1;
        if (__builtin_amdgcn_ballot_w64(tg == tag) == ~0ull) break;
        __builtin_amdgcn_s_sleep(1);
      }
    }
    STAMP(SI);
    for (unsigned spin = 0; spin < SPIN_CAP; ++spin) {
      if (attempt()) break;
    }
  } else {
    STAMP(SI);
  }
  (void)n1; (void)n2; (void)lane; (void)w;
}

__global__ __launch_bounds__(512, 2) void gqa_fused_kernel(const uint16_t* __restrict__ x, const uint16_t* __restrict__ kc,
                                                           const uint16_t* __restrict__ vc, const uint16_t* __restrict__ wq,
                                                           const uint16_t* __restrict__ wo, uint16_t* __restrict__ out,
                                                           unsigned char* __restrict__ ws) {
  const int tid = threadIdx.x;
  const int lane = tid & 63;
  const int w = __builtin_amdgcn_readfirstlane(tid >> 6);
  const int bid = blockIdx.x;
  const int xk = bid & 7, slot = bid >> 3, hp = slot & 1, kh = (slot >> 1) & 1, b = slot >> 2;
  const int kvh = xk;
  const int g = lane >> 4, i16 = lane & 15;
  const int hh = w >> 2, jq = w & 3;
  const int head0 = kvh * 4 + hp * 2;

  STAMPR(0);
  unsigned* epoch_ptr = (unsigned*)(ws + WS_EPOCH + (size_t)b * 256);
  const unsigned epoch_v = __hip_atomic_load(epoch_ptr, __ATOMIC_RELAXED, __HIP_MEMORY_SCOPE_AGENT);

  const char* kbase = (const char*)kc + ((size_t)(b * 8 + kvh) * SK + kh * 1024) * (DH * 2);
  const char* vbase = (const char*)vc + ((size_t)(b * 8 + kvh) * SK + kh * 1024) * (DH * 2);
  const char* wobase = (const char*)wo + ((size_t)head0 * DH * DM + kh * 256) * 2;

  unsigned voffK[4], voffV[2];
#pragma unroll
  for (int i = 0; i < 4; ++i) voffK[i] = (unsigned)(w * 4096 + g * 256 + ((i16 ^ (4 * i + g) ^ ((i & 1) << 3)) << 4));
#pragma unroll
  for (int i = 0; i < 2; ++i) voffV[i] = (unsigned)(w * 4096 + g * 256 + ((i16 ^ (2 * (4 * i + g))) << 4));
  const unsigned w4k = (unsigned)(w * 4096);
  u32x4 qf[4][3], kf[8];
  unsigned ka[4];
#pragma unroll
  for (int s = 0; s < 4; ++s) ka[s] = (32 * jq + i16) * 256 + (((4 * g + s) ^ i16 ^ ((i16 & 4) << 1)) << 4);

  auto do_touch = [&]() {
    if (w == 0) touch((lane & 1) ? ((lane & 2) ? WS_HINT2 : WS_HINT1) + (unsigned)bid * 128u
                                 : WS_IN * (unsigned)(b + 1) + (unsigned)(lane >> 1) * 65536u, ws, TOUCH_OFF);
  };
  {
    do_touch();
    STAMP(42);
    const char* xb = (const char*)x + (size_t)b * SQ * DM * 2;
    const int rot = (slot >> 1) & 7;
    const char* wqb = (const char*)wq + head0 * (DH * 2);
    const unsigned voffQ = (unsigned)((8 * w + (lane >> 5)) * (NQ * 2) + (((lane & 31) ^ (2 * ((lane >> 5) + 4 * (w & 1)))) << 4));
    const int xrow = 6 * w + (lane >> 3);
    const unsigned voffX = (unsigned)(xrow * 1024 + (((lane & 7) ^ ((xrow >> 1) & 7)) << 4));
    auto rq_piece = [&](auto sc, auto pc) {
      constexpr int st = decltype(sc)::value, i = decltype(pc)::value - 1;
      const int tt = (st + rot) & 7;
      if constexpr (i < 0) {
        if (lane < 48) dma_def<0>(voffX, xb + tt * 128, (unsigned)(PB + (st & 3) * 6144 + w * 768));
      } else {
        const char* src = wqb + (size_t)(tt * 64) * (NQ * 2);
        dma_def<i * 1024>(voffQ ^ ((i & 1) * 64u), src + i * (2 * NQ * 2 - 1024), w4k + (st & 3) * SLOT);
      }
    };
    auto rq_all = [&](auto sc) { static_for<0, 5>([&](auto pc) { rq_piece(sc, pc); }); };
    rq_all(IC(0));
    rq_all(IC(1));
    SB();
    STAMP(43);
    SB();
    if (tid < 16) lds_stu(FL_OFF + tid * 4, 0u);
    unsigned aq[2];
    {
      const int q_ = (lane >> 2) & 3, p = lane & 3;
#pragma unroll
      for (int dt = 0; dt < 2; ++dt)
        aq[dt] = (8 * g + q_) * 512 + (((4 * w + 2 * dt + (p >> 1)) ^ (2 * (q_ + 4 * (g & 1)))) << 4) + 8 * (p & 1);
    }
    const unsigned xl = PB + i16 * 128 + ((g ^ (i16 >> 1)) << 4);
    f32x4 acc[2][3];
#pragma unroll
    for (int dt = 0; dt < 2; ++dt)
#pragma unroll
      for (int t = 0; t < 3; ++t) acc[dt][t] = f32x4{0.f, 0.f, 0.f, 0.f};
    u32x4 af[2][2][2], bf[2][2][3];
    auto frag_a = [&](auto sc, int e) {
      constexpr int st = decltype(sc)::value, pp = st & 1;
      constexpr unsigned so = (st & 3) * SLOT;
#pragma unroll
      for (int dt = 0; dt < 2; ++dt) {
        const u32x2 v0 = lds_tr(aq[dt] + so + e * 16384), v1 = lds_tr(aq[dt] + so + e * 16384 + 2048);
        af[pp][e][dt] = u32x4{v0[0], v0[1], v1[0], v1[1]};
      }
    };
    auto frag_b = [&](auto sc, int e) {
      constexpr int st = decltype(sc)::value, pp = st & 1;
      constexpr unsigned xo = (st & 3) * 6144;
#pragma unroll
      for (int t = 0; t < 3; ++t) bf[pp][e][t] = lds_ld128((xl ^ (e * 64u)) + xo + t * 2048);
    };
    auto frags = [&](auto sc) { frag_a(sc, 0); frag_b(sc, 0); frag_a(sc, 1); frag_b(sc, 1); };
    auto qm = [&](int pp, int e, int dt) {
#pragma unroll
      for (int t = 0; t < 3; ++t) acc[dt][t] = mfma(af[pp][e][dt], bf[pp][e][t], acc[dt][t]);
    };
    wait_barrier<5>();
    STAMP(29);
    rq_all(IC(2));
    SB();
    frags(IC(0));
    SB();
    rq_all(IC(3));
    SB();
    static_for<0, 7>([&](auto sc) {
      constexpr int st = decltype(sc)::value, pp = st & 1;
      if constexpr (st <= 4) wait_barrier<10>(); else if constexpr (st == 5) wait_barrier<9>(); else wait_barrier<8>();
      SB();
      STAMP(44 + st);
      auto piece = [&](auto pc) {
        constexpr int p = decltype(pc)::value;
        if constexpr (st <= 3) rq_piece(IC(st + 4), pc);
        else if constexpr (p >= 1) {
          if constexpr (st == 4) dma_nt<(p - 1) * 1024>(voffK[p - 1], kbase, w4k);
          if constexpr (st == 5) dma_nt<(p - 1) * 1024>(voffK[p - 1], kbase + 32768, w4k + SLOT);
          if constexpr (st == 6) dma_nt<(p - 1) * 1024>(voffV[(p - 1) & 1], vbase, w4k + 2 * SLOT);
        }
        SB();
      };
      piece(IC(0));
      piece(IC(1));
      frag_a(IC(st + 1), 0); qm(pp, 0, 0); SB();
      piece(IC(2));
      frag_b(IC(st + 1), 0); qm(pp, 0, 1); SB();
      piece(IC(3));
      frag_a(IC(st + 1), 1); qm(pp, 1, 0); SB();
      piece(IC(4));
      frag_b(IC(st + 1), 1); qm(pp, 1, 1); SB();
    });
    STAMP(30);
    wait_barrier<8>();
#pragma unroll
    for (int c = 0; c < 8; ++c) kf[c] = lds_ld128(ka[c & 3] + (c >> 2) * 4096);
    SB();
    qm(1, 0, 0); qm(1, 0, 1); qm(1, 1, 0); qm(1, 1, 1);
#pragma unroll
    for (int dt = 0; dt < 2; ++dt)
#pragma unroll
      for (int t = 0; t < 3; ++t) {
        const u32x2 v = {pk_bf16(acc[dt][t][0] * SCL, acc[dt][t][1] * SCL), pk_bf16(acc[dt][t][2] * SCL, acc[dt][t][3] * SCL)};
        *(AS3 u32x2*)(size_t)(XIMG + hh * 12288 + t * 4096 + (4 * jq + 2 * dt + (g >> 1)) * 256 + i16 * 16 + (g & 1) * 8) = v;
      }
    STAMP(31);
    lds_barrier();
#pragma unroll
    for (int s = 0; s < 4; ++s)
#pragma unroll
      for (int t = 0; t < 3; ++t) qf[s][t] = lds_ld128(XIMG + hh * 12288 + t * 4096 + (4 * g + s) * 256 + i16 * 16);
  }
  STAMPR(1);

  unsigned va[4];
  {
    const int q_ = (lane >> 2) & 3, p = lane & 3;
#pragma unroll
    for (int dt = 0; dt < 4; ++dt)
      va[dt] = 2 * SLOT + (32 * jq + 4 * g + q_) * 256 + (((8 * (jq & 1) + 2 * (dt ^ (jq & 2)) + (p >> 1)) ^ (2 * ((4 * g + q_) & 7))) << 4) + 8 * (p & 1);
  }
  const unsigned pw = PB + hh * 12288 + (jq >> 1) * 6144 + (jq & 1) * 1024 + lane * 16;

  f32x4 O[4][3];
#pragma unroll
  for (int dt = 0; dt < 4; ++dt)
#pragma unroll
    for (int t = 0; t < 3; ++t) O[dt][t] = f32x4{0.f, 0.f, 0.f, 0.f};
  float lsum[3] = {0.f, 0.f, 0.f};
  f32x4 nm[3];
  f32x4 ps[3];
  const u32x4 ones = {0x3F803F80u, 0x3F803F80u, 0x3F803F80u, 0x3F803F80u};
  const f32x4 zero4 = {0.f, 0.f, 0.f, 0.f};
  const u32x4 zerou = {0u, 0u, 0u, 0u};

  auto tile_max = [&](const f32x4(&S)[2][3], int t) {
    float m = fmaxf(fmaxf(S[0][t][0], S[0][t][1]), S[0][t][2]);
    m = fmaxf(fmaxf(m, S[0][t][3]), S[1][t][0]);
    m = fmaxf(fmaxf(m, S[1][t][1]), S[1][t][2]);
    return fmaxf(m, S[1][t][3]);
  };
  auto head_max = [&](const f32x4(&S)[2][3], float (&m)[3]) {
#pragma unroll
    for (int t = 0; t < 3; ++t) {
      const float v = grp_max(tile_max(S, t));
      if (lane < 16) lds_stf(MX_OFF + ((hh * 4 + jq) * 3 + t) * 64 + lane * 4, v);
    }
    lds_barrier();
#pragma unroll
    for (int t = 0; t < 3; ++t) {
      float v = lds_ldf(MX_OFF + ((hh * 4 + 0) * 3 + t) * 64 + i16 * 4);
#pragma unroll
      for (int j = 1; j < 4; ++j) v = fmaxf(v, lds_ldf(MX_OFF + ((hh * 4 + j) * 3 + t) * 64 + i16 * 4));
      m[t] = v;
    }
  };
  auto p_unit = [&](const f32x4& s) { return u32x2{pk_bf16(ex2(s[0]), ex2(s[1])), pk_bf16(ex2(s[2]), ex2(s[3]))}; };
  auto p_put = [&](int t, u32x2 a, u32x2 c) {
    const u32x4 f = {a[0], a[1], c[0], c[1]};
    lds_st128(pw + t * 2048, f);
    return f;
  };
  auto v_frag = [&](unsigned a) {
    const u32x2 v0 = lds_tr(a), v1 = lds_tr(a + 4096);
    return u32x4{v0[0], v0[1], v1[0], v1[1]};
  };

  unsigned vxb = voffV[0], vyb = voffK[0];
  unsigned wvb = (unsigned)((16 * w + g) * 1024 + ((i16 ^ (2 * g)) << 4));

  f32x4 SA[2][3], SB_[2][3];
  u32x4 Pown[3], Ppar[3], vg[4];
#pragma unroll
  for (int t = 0; t < 3; ++t) Ppar[t] = zerou;
  vg[2] = zerou;
  vg[3] = zerou;
  STAMP(2);
#pragma unroll
  for (int c = 0; c < 8; ++c)
#pragma unroll
    for (int t = 0; t < 3; ++t) SA[c >> 2][t] = mfma(kf[c], qf[c & 3][t], (c & 3) == 0 ? zero4 : SA[c >> 2][t]);
  {
    float m[3];
    head_max(SA, m);
    static_for<0, 4>([&](auto ic) { constexpr int i = decltype(ic)::value; dma_nt<i * 1024>(voffK[i], kbase + 2 * 32768, w4k); });
#pragma unroll
    for (int t = 0; t < 3; ++t) {
      nm[t] = f32x4{-m[t], -m[t], -m[t], -m[t]};
      SA[0][t] += nm[t];
      SA[1][t] += nm[t];
    }
  }

  struct Req { const char* src; unsigned ex, x1, x2, x3, vb; bool shared; };
  auto req_issue = [&](auto ic, const Req& r) {
    constexpr int i = decltype(ic)::value;
    if constexpr (i == 0) dma_0<0>(r.vb, r.src, r.shared);
    else dma_0<i * 1024>(r.vb ^ (i == 1 ? r.x1 : (i == 2 ? r.x2 : r.x3)), r.src + i * r.ex, r.shared);
  };
  auto pv3 = [&](int dt, const u32x4& v, const u32x4 (&p)[3]) {
#pragma unroll
    for (int t = 0; t < 3; ++t) O[dt][t] = mfma(v, p[t], O[dt][t]);
  };
  auto xphase = [&](auto ksc, auto dsc, auto lastc, f32x4(&SC)[2][3], f32x4(&SN)[2][3], const Req& rq, int t) {
    constexpr unsigned ko = decltype(ksc)::value * SLOT;
    constexpr bool LAST = decltype(lastc)::value;
    LDMA(set_m0(w4k + decltype(dsc)::value * SLOT));
    auto qk = [&](auto cc) {
      constexpr int c = decltype(cc)::value, kt = c >> 2, s = c & 3;
      if constexpr (!LAST) {
#pragma unroll
        for (int tt = 0; tt < 3; ++tt) SN[kt][tt] = mfma(kf[c], qf[s][tt], s == 0 ? nm[tt] : SN[kt][tt]);
      }
    };
    if constexpr (!LAST) {
#pragma unroll
      for (int c = 0; c < 4; ++c) kf[c] = lds_ld128(ka[c] + ko);
    }
    SB();
    pv3(2, vg[2], Ppar);
    u32x2 a0 = p_unit(SC[0][0]);
    SB();
    pv3(3, vg[3], Ppar);
    u32x2 a1 = p_unit(SC[1][0]);
    Pown[0] = p_put(0, a0, a1);
    SB();
    if constexpr (!LAST) {
#pragma unroll
      for (int c = 4; c < 8; ++c) kf[c] = lds_ld128(ka[c & 3] + ko + 4096);
    }
    LDMA(req_issue(IC(0), rq));
    SB();
    qk(IC(0));
    a0 = p_unit(SC[0][1]);
    SB();
    qk(IC(1));
    a1 = p_unit(SC[1][1]);
    Pown[1] = p_put(1, a0, a1);
    ps[0] = mfma(ones, Pown[0], zero4);
    SB();
    LDMA(req_issue(IC(1), rq));
    SB();
    qk(IC(2));
    a0 = p_unit(SC[0][2]);
    SB();
    qk(IC(3));
    a1 = p_unit(SC[1][2]);
    Pown[2] = p_put(2, a0, a1);
    ps[1] = mfma(ones, Pown[1], zero4);
    SB();
    LDMA(req_issue(IC(2), rq));
    SB();
    qk(IC(4));
    ps[2] = mfma(ones, Pown[2], zero4);
    SB();
    LDMA(req_issue(IC(3), rq));
    SB();
    qk(IC(5));
    {
      const unsigned u0 = f2u(ps[0][0]), u1 = f2u(ps[1][0]), u2 = f2u(ps[2][0]);
      const unsigned um = max(max(u0, u1), u2);
      if (um > SUM_CAP) lds_stu(FL_OFF + t * 4, 1u);
    }
    SB();
  };
  auto yphase = [&](auto vsc, auto dsc, auto lastc, f32x4(&SC)[2][3], f32x4(&SN)[2][3], const Req& rq, int t) {
    constexpr unsigned vo = (decltype(vsc)::value - 2) * SLOT;
    constexpr bool LAST = decltype(lastc)::value;
    if constexpr (!LAST) LDMA(set_m0(w4k + decltype(dsc)::value * SLOT));
    const unsigned flagv = lds_ldu(FL_OFF + t * 4);
    SB();
    u32x4 vf[4];
#pragma unroll
    for (int dt = 0; dt < 4; ++dt) vf[dt] = v_frag(va[dt] + vo);
    SB();
    if constexpr (!LAST) {
#pragma unroll
      for (int tt = 0; tt < 3; ++tt) SN[1][tt] = mfma(kf[6], qf[2][tt], SN[1][tt]);
#pragma unroll
      for (int tt = 0; tt < 3; ++tt) SN[1][tt] = mfma(kf[7], qf[3][tt], SN[1][tt]);
    }
    SB();
    if (__builtin_amdgcn_readfirstlane(flagv)) {
      float m[3];
      head_max(SC, m);
#pragma unroll
      for (int tt = 0; tt < 3; ++tt) {
        const float d = fmaxf(m[tt], 0.f);
        const float alpha = ex2(-d);
        const f32x4 d4 = {d, d, d, d};
        lsum[tt] *= alpha;
#pragma unroll
        for (int dt = 0; dt < 4; ++dt) O[dt][tt] *= alpha;
        nm[tt] -= d4;
        SC[0][tt] -= d4;
        SC[1][tt] -= d4;
        if constexpr (!LAST) { SN[0][tt] -= d4; SN[1][tt] -= d4; }
        Pown[tt] = p_put(tt, p_unit(SC[0][tt]), p_unit(SC[1][tt]));
        ps[tt] = mfma(ones, Pown[tt], zero4);
      }
      lds_barrier();
    }
    SB();
#pragma unroll
    for (int tt = 0; tt < 3; ++tt) Ppar[tt] = lds_ld128((pw ^ 1024u) + tt * 2048);
#pragma unroll
    for (int dt = 0; dt < 4; ++dt) vg[dt] = v_frag((va[dt] ^ 8192u) + vo);
#pragma unroll
    for (int tt = 0; tt < 3; ++tt) lsum[tt] += ps[tt][0];
    SB();
    pv3(0, vf[0], Pown);
    if constexpr (!LAST) LDMA(req_issue(IC(0), rq));
    SB();
    pv3(1, vf[1], Pown);
    if constexpr (!LAST) LDMA(req_issue(IC(1), rq));
    SB();
    pv3(2, vf[2], Pown);
    if constexpr (!LAST) LDMA(req_issue(IC(2), rq));
    SB();
    pv3(3, vf[3], Pown);
    if constexpr (!LAST) LDMA(req_issue(IC(3), rq));
    SB();
    pv3(0, vg[0], Ppar);
    pv3(1, vg[1], Ppar);
    if constexpr (LAST) {
      pv3(2, vg[2], Ppar);
      pv3(3, vg[3], Ppar);
    }
    SB();
  };
  auto wsrc = [&](int wi) { return wobase + (size_t)(wi >> 1) * 128 * 1024 + (wi & 1) * 256; };
  #define LAUNDER(v) asm volatile("" : "+v"(v))
  auto reqV = [&](int t) { LAUNDER(vxb); return Req{vbase + (size_t)t * 32768, 0u, 0x80u, 0u, 0x80u, vxb, false}; };
  auto reqK = [&](int t) { LAUNDER(vyb); return Req{kbase + (size_t)t * 32768, 0u, 0xC0u, 0x80u, 0x40u, vyb, false}; };
  auto reqW = [&](int wi) { LAUNDER(wvb); return Req{wsrc(wi), 3072u, 0x80u, 0u, 0x80u, wvb, true}; };

#pragma unroll 1
  for (int t2 = 0; t2 < 6; t2 += 2) {
    wait_barrier<8>();
    STAMP(3 + 2 * t2);
    xphase(IC(1), IC(3), std::false_type{}, SA, SB_, reqV(t2 + 1), t2);
    DSTAMP(25);
    wait_barrier<8>();
    STAMP(4 + 2 * t2);
    yphase(IC(2), IC(1), std::false_type{}, SA, SB_, reqK(t2 + 3), t2);
    DSTAMP(26);
    wait_barrier<8>();
    STAMP(5 + 2 * t2);
    xphase(IC(0), IC(2), std::false_type{}, SB_, SA, reqV(t2 + 2), t2 + 1);
    Req ry = t2 == 4 ? reqW(0) : reqK(t2 + 4);
    ry.shared = false;
    DSTAMP(27);
    wait_barrier<8>();
    STAMP(6 + 2 * t2);
    yphase(IC(3), IC(0), std::false_type{}, SB_, SA, ry, t2 + 1);
    DSTAMP(28);
  }
  wait_barrier<8>();
  STAMP(15);
  xphase(IC(1), IC(3), std::false_type{}, SA, SB_, reqV(7), 6);
  wait_barrier<8>();
  STAMP(16);
  yphase(IC(2), IC(1), std::false_type{}, SA, SB_, reqW(1), 6);
  wait_barrier<8>();
  STAMP(17);
  xphase(IC(0), IC(2), std::true_type{}, SB_, SA, reqW(2), 7);
  wait_barrier<8>();
  STAMP(18);
  yphase(IC(3), IC(3), std::true_type{}, SB_, SA, reqW(3), 7);

#pragma unroll
  for (int t = 0; t < 3; ++t)
    if (lane < 16) lds_stf(LS_OFF + ((hh * 4 + jq) * 3 + t) * 64 + lane * 4, lsum[t]);
  wait_barrier<0>();
  const unsigned tag = (unsigned)__builtin_amdgcn_readfirstlane(epoch_v) + 1u;
  SB();
  STAMPR(19);
  set_m0(w4k + 3 * SLOT);
  static_for<0, 4>([&](auto ic) { req_issue(ic, reqW(3)); });
  const unsigned ow = PB + hh * 12288 + (2 * (jq & 1) + (jq >> 1)) * 3072 + lane * 16;
  const unsigned ox = PB + hh * 12288 + (2 * (jq & 1) + ((jq >> 1) ^ 1)) * 3072 + lane * 16;
  f32x4 Om[2][3];
  float lt[3];
  {
#pragma unroll
    for (int t = 0; t < 3; ++t) lds_st128(ow + t * 1024, __builtin_bit_cast(u32x4, O[2][t]));
#pragma unroll
    for (int t = 0; t < 3; ++t) {
      float s = 0.f;
#pragma unroll
      for (int j = 0; j < 4; ++j) s += lds_ldf(LS_OFF + ((hh * 4 + j) * 3 + t) * 64 + i16 * 4);
      lt[t] = s;
    }
    lds_barrier();
#pragma unroll
    for (int t = 0; t < 3; ++t) Om[0][t] = O[0][t] + __builtin_bit_cast(f32x4, lds_ld128(ox + t * 1024));
    SB();
#pragma unroll
    for (int t = 0; t < 3; ++t) lds_st128(ox + t * 1024, __builtin_bit_cast(u32x4, O[3][t]));
    lds_barrier();
#pragma unroll
    for (int t = 0; t < 3; ++t) Om[1][t] = O[1][t] + __builtin_bit_cast(f32x4, lds_ld128(ow + t * 1024));
  }

  STAMP(34);
  const __amdgpu_buffer_rsrc_t ws_rs = __builtin_amdgcn_make_buffer_rsrc((void*)ws, 0, WS_END, 0x00020000);
  const unsigned reg1 = WS_IN * (unsigned)(b + 1), reg2 = reg1 + WS_IN2;
  const unsigned lbid = (unsigned)bid & 31u;

  float lam[3];
  u32x4 mine[3];
#pragma unroll
  for (int t = 0; t < 3; ++t) {
    const float inv = __builtin_amdgcn_rcpf(lt[t]);
    lam[t] = __builtin_amdgcn_logf(lt[t]) - nm[t][0];
#pragma unroll
    for (int dt = 0; dt < 2; ++dt) Om[dt][t] *= inv;
    mine[t] = u32x4{pk_bf16(Om[0][t][0], Om[0][t][1]), pk_bf16(Om[0][t][2], Om[0][t][3]), pk_bf16(Om[1][t][0], Om[1][t][1]),
                    pk_bf16(Om[1][t][2], Om[1][t][3])};
  }
  {
    const float l0 = lam[0], l1 = lam[1], l2 = lam[2];
    const unsigned lw = __builtin_bit_cast(unsigned, (lane & 32) ? l2 : ((lane & 16) ? l1 : l0));
    unsigned h1, h2;
    hash13(mine[0], mine[1], mine[2], lw, tag, h1, h2);
    const unsigned dst = reg1 + (lbid ^ 16u) * 32768u + (unsigned)(w * 4096 + lane * 16);
    __builtin_amdgcn_raw_buffer_store_b128(mine[0], ws_rs, dst, 0, 16);
    __builtin_amdgcn_raw_buffer_store_b128(mine[1], ws_rs, dst + 1024, 0, 16);
    __builtin_amdgcn_raw_buffer_store_b128(mine[2], ws_rs, dst + 2048, 0, 16);
    __builtin_amdgcn_raw_buffer_store_b128(u32x4{lw, h1, h2, tag}, ws_rs, dst + 3072, 0, 16);
    SB();
    if (lane == 0) __builtin_amdgcn_raw_buffer_store_b32(tag, ws_rs, WS_HINT1 + (unsigned)((bid ^ 16) * 128 + w * 4), 0, 16);
  }
  STAMP(35);
  unsigned wa_[2];
  {
    const int q_ = (lane >> 2) & 3, p = lane & 3;
#pragma unroll
    for (int dt = 0; dt < 2; ++dt)
      wa_[dt] = hh * SLOT + (4 * g + q_) * 256 + (((4 * jq + 2 * dt + (p >> 1)) ^ (2 * ((4 * g + q_) & 7))) << 4) + 8 * (p & 1);
  }
  u32x4 wf[8][2];
#pragma unroll
  for (int st = 0; st < 4; ++st)
#pragma unroll
    for (int dt = 0; dt < 2; ++dt) wf[st][dt] = v_frag(wa_[dt] + st * 8192);
  SB();
  u32x4 th[4];
  STAMP(36);
  recv4<1024, 37, HOP1_AUX>(ws_rs, WS_HINT1 + (unsigned)(bid * 128 + w * 4), reg1 + lbid * 32768u + (unsigned)(w * 4096 + lane * 16), tag, th, lane, w);
  STAMPR(20);
  {
    const unsigned u = th[3][0];
    auto a = __builtin_amdgcn_permlane32_swap(u, u, false, false);
    auto c0 = __builtin_amdgcn_permlane16_swap(a[0], a[0], false, false);
    auto c1 = __builtin_amdgcn_permlane16_swap(a[1], a[1], false, false);
    const float lb[3] = {u2f(c0[0]), u2f(c0[1]), u2f(c1[0])};
#pragma unroll
    for (int t = 0; t < 3; ++t) {
      const float mx = fmaxf(lam[t], lb[t]);
      float wa = ex2(lam[t] - mx), wb = ex2(lb[t] - mx);
      const float inv = __builtin_amdgcn_rcpf(wa + wb);
      wa *= inv;
      wb *= inv;
      float m[8];
#pragma unroll
      for (int k = 0; k < 4; ++k) {
        m[2 * k] = wa * Om[k >> 1][t][2 * (k & 1)] + wb * bf_lo(th[t][k]);
        m[2 * k + 1] = wa * Om[k >> 1][t][2 * (k & 1) + 1] + wb * bf_hi(th[t][k]);
      }
      lds_st128(ow + t * 1024, u32x4{pk_bf16(m[0], m[1]), pk_bf16(m[2], m[3]), pk_bf16(m[4], m[5]), pk_bf16(m[6], m[7])});
    }
  }
  wait_barrier<0>();
  STAMP(21);

  const unsigned pr = PB + lane * 16;
  f32x4 A[2][3];
#pragma unroll
  for (int dt = 0; dt < 2; ++dt)
#pragma unroll
    for (int t = 0; t < 3; ++t) A[dt][t] = zero4;
  {
    u32x4 pf[8][3];
#pragma unroll
    for (int st = 0; st < 4; ++st)
#pragma unroll
      for (int t = 0; t < 3; ++t) pf[st][t] = lds_ld128(pr + st * 3072 + t * 1024);
#pragma unroll
    for (int st = 4; st < 8; ++st) {
#pragma unroll
      for (int dt = 0; dt < 2; ++dt) wf[st][dt] = v_frag(wa_[dt] + 2 * SLOT + (st - 4) * 8192);
#pragma unroll
      for (int t = 0; t < 3; ++t) pf[st][t] = lds_ld128(pr + st * 3072 + t * 1024);
    }
    SB();
#pragma unroll
    for (int st = 0; st < 8; ++st)
#pragma unroll
      for (int dt = 0; dt < 2; ++dt)
#pragma unroll
        for (int t = 0; t < 3; ++t) A[dt][t] = mfma(wf[st][dt], pf[st][t], A[dt][t]);
  }
  STAMP(22);

  {
    u32x4 f[3];
#pragma unroll
    for (int t = 0; t < 3; ++t)
      f[t] = u32x4{pk_bf16(A[0][t][0], A[0][t][1]), pk_bf16(A[0][t][2], A[0][t][3]), pk_bf16(A[1][t][0], A[1][t][1]),
                   pk_bf16(A[1][t][2], A[1][t][3])};
    unsigned h1, h2;
    hash13(f[0], f[1], f[2], 0u, tag, h1, h2);
    const int rho = 2 * w + (i16 >> 3);
    const int rbid = (rho >> 1) + 8 * ((rho & 1) + 2 * kh + 4 * b);
    const int pidx = kvh * 2 + hp;
    const unsigned dst = reg2 + (unsigned)((rbid & 31) * 32768 + pidx * 2048 + (8 * g + (i16 & 7)) * 16);
    __builtin_amdgcn_raw_buffer_store_b128(f[0], ws_rs, dst, 0, 16);
    __builtin_amdgcn_raw_buffer_store_b128(f[1], ws_rs, dst + 512, 0, 16);
    __builtin_amdgcn_raw_buffer_store_b128(f[2], ws_rs, dst + 1024, 0, 16);
    __builtin_amdgcn_raw_buffer_store_b128(u32x4{0u, h1, h2, tag}, ws_rs, dst + 1536, 0, 16);
    SB();
    if ((lane & 55) == 0) __builtin_amdgcn_raw_buffer_store_b32(tag, ws_rs, WS_HINT2 + (unsigned)(rbid * 128 + pidx * 4), 0, 16);
  }
  {
    STAMP(39);
    recv4<512, 40, HOP2_AUX>(ws_rs, WS_HINT2 + (unsigned)(bid * 128 + (tid >> 5) * 4), reg2 + lbid * 32768u + (unsigned)((tid >> 5) * 2048 + (tid & 31) * 16), tag, th, lane, w);
    STAMPR(23);
#pragma unroll
    for (int t = 0; t < 3; ++t) {
      f32x4 r;
#pragma unroll
      for (int k = 0; k < 2; ++k) {
        const float a0 = half_sum(bf_lo(th[t][k])), a1 = half_sum(bf_hi(th[t][k]));
        const float b0 = half_sum(bf_lo(th[t][2 + k])), b1 = half_sum(bf_hi(th[t][2 + k]));
        r[2 * k] = lane < 32 ? a0 : b0;
        r[2 * k + 1] = lane < 32 ? a1 : b1;
      }
      lds_st128(w * 3072 + t * 1024 + lane * 16, __builtin_bit_cast(u32x4, r));
    }
  }
  lds_barrier();
  if (tid < 192) {
    const int t = tid >> 6;
    f32x4 s = __builtin_bit_cast(f32x4, lds_ld128(t * 1024 + lane * 16));
#pragma unroll
    for (int v = 1; v < 8; ++v) s += __builtin_bit_cast(f32x4, lds_ld128(v * 3072 + t * 1024 + lane * 16));
    const int rho = xk * 2 + hp;
    const int nt = lane >> 5, l32 = lane & 31, gg = l32 >> 3, q = 8 * (rho & 1) + (l32 & 7);
    const int col = 256 * kh + 32 * (rho >> 1) + 16 * nt + 4 * gg;
    u32x2 o = {pk_bf16(s[0], s[1]), pk_bf16(s[2], s[3])};
    *(u32x2*)(out + ((size_t)b * SQ + 16 * t + q) * DM + col) = o;
  }
  STAMPR(24);
  STAMP_DUMP();
  if (tid == 0 && xk == 0 && hp == 0 && kh == 0) __hip_atomic_store(epoch_ptr, tag, __ATOMIC_RELAXED, __HIP_MEMORY_SCOPE_AGENT);
}

}

extern "C" void kernel_launch(void* const* d_in, const int* in_sizes, int n_in, void* d_out, int out_size, void* d_ws,
                              size_t ws_size, hipStream_t stream) {
  (void)in_sizes; (void)n_in; (void)out_size; (void)ws_size;
  (void)hipFuncSetAttribute((const void*)gqa_fused_kernel, hipFuncAttributeMaxDynamicSharedMemorySize, LDS_BYTES);
  gqa_fused_kernel<<<256, 512, LDS_BYTES, stream>>>((const uint16_t*)d_in[0], (const uint16_t*)d_in[1], (const uint16_t*)d_in[2],
                                                    (const uint16_t*)d_in[3], (const uint16_t*)d_in[4], (uint16_t*)d_out,
                                                    (unsigned char*)d_ws);
}
```

```cpp
#include <hip/hip_runtime.h>
#include <hip/hip_bf16.h>
#include <stdint.h>
#include <type_traits>

#define AS3 __attribute__((address_space(3)))
typedef __attribute__((ext_vector_type(8))) __bf16 bf16x8;
typedef __attribute__((ext_vector_type(2))) __bf16 bf16x2;
typedef __attribute__((ext_vector_type(4))) float f32x4;
typedef __attribute__((ext_vector_type(2))) float f32x2;
typedef __attribute__((ext_vector_type(4))) unsigned u32x4;
typedef __attribute__((ext_vector_type(2))) unsigned u32x2;
typedef __attribute__((ext_vector_type(4))) short i16x4;

#define STAMP(i) do {} while (0)
#define STAMPR(i) do {} while (0)
#define STAMP_DUMP() do { __builtin_amdgcn_s_waitcnt(0xc07f);} while (0)
#define SB() __builtin_amdgcn_sched_barrier(0)
#define IC(v) std::integral_constant<int, (v)>{}
#define HOP1_AUX 0
#define HOP2_AUX 0
#define MAPK 3
#define TOUCH_AT 1
#define KVPOL " nt"
#define LDMA(x) x
#define DSTAMP(i) do {} while (0)

namespace {

constexpr int SQ = 48, DM = 512, SK = 2048, DH = 128, NQ = 4096;

constexpr unsigned SLOT = 32768;
constexpr unsigned PB = 131072;
constexpr unsigned MISC = 155648;
constexpr unsigned MX_OFF = MISC;
constexpr unsigned LS_OFF = MISC + 1536;
constexpr unsigned FL_OFF = MISC + 3072;
constexpr unsigned TOUCH_OFF = MISC + 3328;
constexpr unsigned XIMG = 98304;
constexpr unsigned LDS_BYTES = 163840;

constexpr unsigned WS_EPOCH = 0;
constexpr unsigned WS_HINT1 = 4096;
constexpr unsigned WS_HINT2 = WS_HINT1 + 32768;
constexpr unsigned WS_IN = 2u << 20;
constexpr unsigned WS_IN2 = 1u << 20;
constexpr unsigned WS_END = WS_IN * 9;

constexpr float SCL = 0.08838834764831845f * 1.4426950408889634f;
constexpr unsigned SUM_CAP = 0x47800000u;
constexpr unsigned SPIN_CAP = 1u << 16;

__device__ __forceinline__ u32x4 lds_ld128(unsigned a) { return *(const AS3 u32x4*)(size_t)a; }
__device__ __forceinline__ void lds_st128(unsigned a, u32x4 v) { *(AS3 u32x4*)(size_t)a = v; }
__device__ __forceinline__ float lds_ldf(unsigned a) { return *(const AS3 float*)(size_t)a; }
__device__ __forceinline__ void lds_stf(unsigned a, float v) { *(AS3 float*)(size_t)a = v; }
__device__ __forceinline__ unsigned lds_ldu(unsigned a) { return *(const AS3 unsigned*)(size_t)a; }
__device__ __forceinline__ void lds_stu(unsigned a, unsigned v) { *(AS3 unsigned*)(size_t)a = v; }
__device__ __forceinline__ u32x2 lds_tr(unsigned a) {
  i16x4 r = __builtin_amdgcn_ds_read_tr16_b64_v4i16((AS3 i16x4*)(size_t)a);
  return __builtin_bit_cast(u32x2, r);
}
__device__ __forceinline__ f32x4 mfma(u32x4 a, u32x4 b, f32x4 c) {
  return __builtin_amdgcn_mfma_f32_16x16x32_bf16(__builtin_bit_cast(bf16x8, a), __builtin_bit_cast(bf16x8, b), c, 0, 0, 0);
}
__device__ __forceinline__ unsigned pk_bf16(float lo, float hi) {
  f32x2 v = {lo, hi};
  bf16x2 r = __builtin_convertvector(v, bf16x2);
  return __builtin_bit_cast(unsigned, r);
}
__device__ __forceinline__ float u2f(unsigned u) { return __builtin_bit_cast(float, u); }
__device__ __forceinline__ unsigned f2u(float f) { return __builtin_bit_cast(unsigned, f); }
__device__ __forceinline__ float bf_lo(unsigned u) { return __builtin_bit_cast(float, u << 16); }
__device__ __forceinline__ float bf_hi(unsigned u) { return __builtin_bit_cast(float, u & 0xffff0000u); }
__device__ __forceinline__ float ex2(float v) { return __builtin_amdgcn_exp2f(v); }

template <int OFF>
__device__ __forceinline__ void dma_nt(unsigned voff, const void* sbase, unsigned m0v) {
  asm volatile("s_mov_b32 m0, %2\n\ts_nop 0\n\tglobal_load_lds_dwordx4 %0, %1 offset:%3" KVPOL
               :: "v"(voff), "s"(sbase), "s"(m0v), "n"(OFF) : "memory");
}
template <int OFF>
__device__ __forceinline__ void dma_def(unsigned voff, const void* sbase, unsigned m0v) {
  asm volatile("s_mov_b32 m0, %2\n\ts_nop 0\n\tglobal_load_lds_dwordx4 %0, %1 offset:%3"
               :: "v"(voff), "s"(sbase), "s"(m0v), "n"(OFF) : "memory");
}
__device__ __forceinline__ void set_m0(unsigned m0v) { asm volatile("s_mov_b32 m0, %0\n\ts_nop 0" ::"s"(m0v) : "memory"); }
template <int OFF>
__device__ __forceinline__ void dma_0(unsigned vb, const void* sbase, bool shared) {
  if (shared) asm volatile("global_load_lds_dwordx4 %0, %1 offset:%2" ::"v"(vb), "s"(sbase), "n"(OFF) : "memory");
  else asm volatile("global_load_lds_dwordx4 %0, %1 offset:%2" KVPOL ::"v"(vb), "s"(sbase), "n"(OFF) : "memory");
}
__device__ __forceinline__ void touch(unsigned voff, const void* sbase, unsigned m0v) {
  asm volatile("s_mov_b32 m0, %2\n\ts_nop 0\n\tglobal_load_lds_dword %0, %1 sc1" ::"v"(voff), "s"(sbase), "s"(m0v) : "memory");
}
template <int I, int N, class F>
__device__ __forceinline__ void static_for(F&& f) {
  if constexpr (I < N) {
    f(std::integral_constant<int, I>{});
    static_for<I + 1, N>(f);
  }
}
template <int N>
__device__ __forceinline__ void wait_barrier() {
  asm volatile("s_waitcnt vmcnt(%0) lgkmcnt(0)\n\ts_barrier" ::"n"(N) : "memory");
}
__device__ __forceinline__ void lds_barrier() { asm volatile("s_waitcnt lgkmcnt(0)\n\ts_barrier" ::: "memory"); }

__device__ __forceinline__ float grp_max(float v) {
  unsigned u = __builtin_bit_cast(unsigned, v);
  auto a = __builtin_amdgcn_permlane16_swap(u, u, false, false);
  v = fmaxf(u2f(a[0]), u2f(a[1]));
  u = __builtin_bit_cast(unsigned, v);
  auto c = __builtin_amdgcn_permlane32_swap(u, u, false, false);
  return fmaxf(u2f(c[0]), u2f(c[1]));
}
__device__ __forceinline__ float half_sum(float v) {
  unsigned u = __builtin_bit_cast(unsigned, v);
  auto c = __builtin_amdgcn_permlane32_swap(u, u, false, false);
  return u2f(c[0]) + u2f(c[1]);
}

template <int R>
__device__ __forceinline__ unsigned rot(unsigned h) { return __builtin_amdgcn_alignbit(h, h, R); }
__device__ __forceinline__ void hash13(const u32x4& a, const u32x4& b, const u32x4& c, unsigned d, unsigned tag,
                                       unsigned& h1, unsigned& h2) {
  auto x3 = [](unsigned p, unsigned q, unsigned r) { return __builtin_amdgcn_bitop3_b32(p, q, r, 0x96); };
  const unsigned x0 = x3(a[0], rot<5>(a[1]), rot<10>(a[2])), x1 = x3(rot<15>(a[3]), rot<20>(b[0]), rot<25>(b[1]));
  const unsigned x2 = x3(rot<30>(b[2]), rot<3>(b[3]), rot<8>(c[0])), x4 = x3(rot<13>(c[1]), rot<18>(c[2]), rot<23>(c[3]));
  h1 = x3(x3(x0, x1, x2), x4, rot<28>(d) ^ (tag * 0x9E3779B1u + 0x7F4A7C15u));
  h2 = (a[0] + a[1] + a[2]) + (a[3] + b[0] + b[1]) + (b[2] + b[3] + c[0]) + (c[1] + c[2] + c[3]) + (d + 2u * tag + 1u);
}

template <int STR, int SI, int MODE>
__device__ __forceinline__ void recv4(const __amdgpu_buffer_rsrc_t& rs, unsigned hint, unsigned src, unsigned tag, u32x4 (&th)[4], int lane, int w) {
  unsigned n1 = 0, n2 = 0;
  auto attempt = [&]() {
    asm volatile("" ::: "memory");
#pragma unroll
    for (int k = 0; k < 4; ++k) th[k] = __builtin_amdgcn_raw_buffer_load_b128(rs, src + k * STR, 0, 16);
    unsigned h1, h2;
    hash13(th[0], th[1], th[2], th[3][0], tag, h1, h2);
    const bool ok = (h1 == th[3][1]) & (h2 == th[3][2]);
    ++n2;
    return __builtin_amdgcn_ballot_w64(ok) == ~0ull;
  };
  bool done = false;
  if constexpr (MODE & 1) done = attempt();
  if (!done) {
    if constexpr (!(MODE & 2)) {
#pragma unroll 1
      for (unsigned s2 = 0; s2 < SPIN_CAP; ++s2) {
        asm volatile("" ::: "memory");
        const unsigned tg = __builtin_amdgcn_raw_buffer_load_b32(rs, hint, 0, 16);
        ++n1;
        if (__builtin_amdgcn_ballot_w64(tg == tag) == ~0ull) break;
        __builtin_amdgcn_s_sleep(1);
      }
    }
    STAMP(SI);
#pragma unroll 1
    for (unsigned spin = 0; spin < SPIN_CAP; ++spin) {
      if (attempt()) break;
    }
  } else {
    STAMP(SI);
  }
  (void)n1; (void)n2; (void)lane; (void)w;
}

__global__ __launch_bounds__(512, 2) void gqa_fused_kernel(const uint16_t* __restrict__ x, const uint16_t* __restrict__ kc,
                                                           const uint16_t* __restrict__ vc, const uint16_t* __restrict__ wq,
                                                           const uint16_t* __restrict__ wo, uint16_t* __restrict__ out,
                                                           unsigned char* __restrict__ ws) {
  const int tid = threadIdx.x;
  const int lane = tid & 63;
  const int w = __builtin_amdgcn_readfirstlane(tid >> 6);
  const int bid = blockIdx.x;
  const int xk = bid & 7, slot = bid >> 3, hp = slot & 1, kh = (slot >> 1) & 1, b = slot >> 2;
  const int kvh = (xk & 4) | ((xk + b) & 3);
  const int g = lane >> 4, i16 = lane & 15;
  const int hh = w >> 2, jq = w & 3;
  const int head0 = kvh * 4 + hp * 2;

  STAMPR(0);
  unsigned* epoch_ptr = (unsigned*)(ws + WS_EPOCH + (size_t)b * 256);

  const char* kbase = (const char*)kc + ((size_t)(b * 8 + kvh) * SK + kh * 1024) * (DH * 2);
  const char* vbase = (const char*)vc + ((size_t)(b * 8 + kvh) * SK + kh * 1024) * (DH * 2);
  const char* wobase = (const char*)wo + ((size_t)head0 * DH * DM + kh * 256) * 2;

  unsigned voffK[4], voffV[2];
#pragma unroll
  for (int i = 0; i < 4; ++i) voffK[i] = (unsigned)(w * 4096 + g * 256 + ((i16 ^ (4 * i + g) ^ ((i & 1) << 3)) << 4));
#pragma unroll
  for (int i = 0; i < 2; ++i) voffV[i] = (unsigned)(w * 4096 + g * 256 + ((i16 ^ (2 * (4 * i + g))) << 4));
  const unsigned w4k = (unsigned)(w * 4096);
  u32x4 qf[4][3], kf[8];
  f32x4 O[4][3];
  float lsum[3];
  u32x4 Ppar[3], vg[4];
  unsigned ka[4];
#pragma unroll
  for (int s = 0; s < 4; ++s) ka[s] = (32 * jq + i16) * 256 + (((4 * g + s) ^ i16 ^ ((i16 & 4) << 1)) << 4);

  auto do_touch = [&]() {
    if (w == 0) touch((lane & 1) ? ((lane & 2) ? WS_HINT2 : WS_HINT1) + (unsigned)bid * 128u
                                 : WS_IN * (unsigned)(b + 1) + (unsigned)(lane >> 1) * 65536u, ws, TOUCH_OFF);
  };
  {
    STAMP(42);
    const char* xb = (const char*)x + (size_t)b * SQ * DM * 2;
    const int rot = (slot >> 1) & 7;
    const char* wqb = (const char*)wq + head0 * (DH * 2);
    const unsigned voffQ = (unsigned)((8 * w + (lane >> 5)) * (NQ * 2) + (((lane & 31) ^ (2 * ((lane >> 5) + 4 * (w & 1)))) << 4));
    const int xrow = 6 * w + (lane >> 3);
    const unsigned voffX = (unsigned)(xrow * 1024 + (((lane & 7) ^ ((xrow >> 1) & 7)) << 4));
    auto rq_piece = [&](auto sc, auto pc) {
      constexpr int st = decltype(sc)::value, i = decltype(pc)::value - 1;
      const int tt = (st + rot) & 7;
      if constexpr (i < 0) {
        if (lane < 48) dma_def<0>(voffX, xb + tt * 128, (unsigned)(PB + (st & 3) * 6144 + w * 768));
      } else {
        const char* src = wqb + (size_t)(tt * 64) * (NQ * 2);
        dma_def<i * 1024>(voffQ ^ ((i & 1) * 64u), src + i * (2 * NQ * 2 - 1024), w4k + (st & 3) * SLOT);
      }
    };
    auto rq_all = [&](auto sc) { static_for<0, 5>([&](auto pc) { rq_piece(sc, pc); }); };
    rq_all(IC(0));
    rq_all(IC(1));
    SB();
    STAMP(43);
    SB();
    if (tid < 16) lds_stu(FL_OFF + tid * 4, 0u);
#pragma unroll
    for (int t = 0; t < 3; ++t) {
#pragma unroll
      for (int dt = 0; dt < 4; ++dt) {
        O[dt][t] = f32x4{0.f, 0.f, 0.f, 0.f};
        asm volatile("" : "+v"(O[dt][t]));
      }
      lsum[t] = 0.f;
      Ppar[t] = u32x4{0u, 0u, 0u, 0u};
      asm volatile("" : "+v"(lsum[t]), "+v"(Ppar[t]));
    }
    vg[2] = u32x4{0u, 0u, 0u, 0u};
    vg[3] = u32x4{0u, 0u, 0u, 0u};
    asm volatile("" : "+v"(vg[2]), "+v"(vg[3]));
    unsigned aq[2];
    {
      const int q_ = (lane >> 2) & 3, p = lane & 3;
#pragma unroll
      for (int dt = 0; dt < 2; ++dt)
        aq[dt] = (8 * g + q_) * 512 + (((4 * w + 2 * dt + (p >> 1)) ^ (2 * (q_ + 4 * (g & 1)))) << 4) + 8 * (p & 1);
    }
    const unsigned xl = PB + i16 * 128 + ((g ^ (i16 >> 1)) << 4);
    f32x4 acc[2][3];
#pragma unroll
    for (int dt = 0; dt < 2; ++dt)
#pragma unroll
      for (int t = 0; t < 3; ++t) acc[dt][t] = f32x4{0.f, 0.f, 0.f, 0.f};
    u32x4 af[2][2][2], bf[2][2][3];
    auto frag_a = [&](auto sc, int e) {
      constexpr int st = decltype(sc)::value, pp = st & 1;
      constexpr unsigned so = (st & 3) * SLOT;
#pragma unroll
      for (int dt = 0; dt < 2; ++dt) {
        const u32x2 v0 = lds_tr(aq[dt] + so + e * 16384), v1 = lds_tr(aq[dt] + so + e * 16384 + 2048);
        af[pp][e][dt] = u32x4{v0[0], v0[1], v1[0], v1[1]};
      }
    };
    auto frag_b = [&](auto sc, int e) {
      constexpr int st = decltype(sc)::value, pp = st & 1;
      constexpr unsigned xo = (st & 3) * 6144;
#pragma unroll
      for (int t = 0; t < 3; ++t) bf[pp][e][t] = lds_ld128((xl ^ (e * 64u)) + xo + t * 2048);
    };
    auto frags = [&](auto sc) { frag_a(sc, 0); frag_b(sc, 0); frag_a(sc, 1); frag_b(sc, 1); };
    auto qm = [&](int pp, int e, int dt) {
#pragma unroll
      for (int t = 0; t < 3; ++t) acc[dt][t] = mfma(af[pp][e][dt], bf[pp][e][t], acc[dt][t]);
    };
    wait_barrier<5>();
    STAMP(29);
    rq_all(IC(2));
    SB();
    frags(IC(0));
    SB();
    rq_all(IC(3));
    SB();
    static_for<0, 7>([&](auto sc) {
      constexpr int st = decltype(sc)::value, pp = st & 1;
      if constexpr (st <= 4) wait_barrier<10>(); else if constexpr (st == 5) wait_barrier<9>(); else wait_barrier<8>();
      SB();
      STAMP(44 + st);
      auto piece = [&](auto pc) {
        constexpr int p = decltype(pc)::value;
        if constexpr (st <= 3) rq_piece(IC(st + 4), pc);
        else if constexpr (p >= 1) {
          if constexpr (st == 4) dma_nt<(p - 1) * 1024>(voffK[p - 1], kbase, w4k);
          if constexpr (st == 5) dma_nt<(p - 1) * 1024>(voffK[p - 1], kbase + 32768, w4k + SLOT);
          if constexpr (st == 6) dma_nt<(p - 1) * 1024>(voffV[(p - 1) & 1], vbase, w4k + 2 * SLOT);
        }
        SB();
      };
      piece(IC(0));
      piece(IC(1));
      frag_a(IC(st + 1), 0); qm(pp, 0, 0); SB();
      piece(IC(2));
      frag_b(IC(st + 1), 0); qm(pp, 0, 1); SB();
      piece(IC(3));
      frag_a(IC(st + 1), 1); qm(pp, 1, 0); SB();
      piece(IC(4));
      frag_b(IC(st + 1), 1); qm(pp, 1, 1); SB();
    });
    STAMP(30);
    wait_barrier<8>();
#pragma unroll
    for (int c = 0; c < 8; ++c) kf[c] = lds_ld128(ka[c & 3] + (c >> 2) * 4096);
    SB();
    qm(1, 0, 0); qm(1, 0, 1); qm(1, 1, 0); qm(1, 1, 1);
#pragma unroll
    for (int dt = 0; dt < 2; ++dt)
#pragma unroll
      for (int t = 0; t < 3; ++t) {
        const u32x2 v = {pk_bf16(acc[dt][t][0] * SCL, acc[dt][t][1] * SCL), pk_bf16(acc[dt][t][2] * SCL, acc[dt][t][3] * SCL)};
        *(AS3 u32x2*)(size_t)(XIMG + hh * 12288 + t * 4096 + (4 * jq + 2 * dt + (g >> 1)) * 256 + i16 * 16 + (g & 1) * 8) = v;
      }
    STAMP(31);
    lds_barrier();
#pragma unroll
    for (int s = 0; s < 4; ++s)
#pragma unroll
      for (int t = 0; t < 3; ++t) qf[s][t] = lds_ld128(XIMG + hh * 12288 + t * 4096 + (4 * g + s) * 256 + i16 * 16);
  }
  STAMPR(1);

  unsigned va[4];
  {
    const int q_ = (lane >> 2) & 3, p = lane & 3;
#pragma unroll
    for (int dt = 0; dt < 4; ++dt)
      va[dt] = 2 * SLOT + (32 * jq + 4 * g + q_) * 256 + (((8 * (jq & 1) + 2 * (dt ^ (jq & 2)) + (p >> 1)) ^ (2 * ((4 * g + q_) & 7))) << 4) + 8 * (p & 1);
  }
  const unsigned pw = PB + hh * 12288 + (jq >> 1) * 6144 + (jq & 1) * 1024 + lane * 16;

  f32x4 nm[3];
  f32x4 ps[3];
  const u32x4 ones = {0x3F803F80u, 0x3F803F80u, 0x3F803F80u, 0x3F803F80u};
  const f32x4 zero4 = {0.f, 0.f, 0.f, 0.f};

  auto tile_max = [&](const f32x4(&S)[2][3], int t) {
    float m = fmaxf(fmaxf(S[0][t][0], S[0][t][1]), S[0][t][2]);
    m = fmaxf(fmaxf(m, S[0][t][3]), S[1][t][0]);
    m = fmaxf(fmaxf(m, S[1][t][1]), S[1][t][2]);
    return fmaxf(m, S[1][t][3]);
  };
  auto head_max = [&](const f32x4(&S)[2][3], float (&m)[3]) {
#pragma unroll
    for (int t = 0; t < 3; ++t) {
      const float v = grp_max(tile_max(S, t));
      if (lane < 16) lds_stf(MX_OFF + ((hh * 4 + jq) * 3 + t) * 64 + lane * 4, v);
    }
    lds_barrier();
#pragma unroll
    for (int t = 0; t < 3; ++t) {
      float v = lds_ldf(MX_OFF + ((hh * 4 + 0) * 3 + t) * 64 + i16 * 4);
#pragma unroll
      for (int j = 1; j < 4; ++j) v = fmaxf(v, lds_ldf(MX_OFF + ((hh * 4 + j) * 3 + t) * 64 + i16 * 4));
      m[t] = v;
    }
  };
  auto p_unit = [&](const f32x4& s) { return u32x2{pk_bf16(ex2(s[0]), ex2(s[1])), pk_bf16(ex2(s[2]), ex2(s[3]))}; };
  auto p_put = [&](int t, u32x2 a, u32x2 c) {
    const u32x4 f = {a[0], a[1], c[0], c[1]};
    lds_st128(pw + t * 2048, f);
    return f;
  };
  auto v_frag = [&](unsigned a) {
    const u32x2 v0 = lds_tr(a), v1 = lds_tr(a + 4096);
    return u32x4{v0[0], v0[1], v1[0], v1[1]};
  };

  unsigned vxb = voffV[0], vyb = voffK[0];
  unsigned wvb = (unsigned)((16 * w + g) * 1024 + ((i16 ^ (2 * g)) << 4));

  f32x4 SA[2][3], SB_[2][3];
  u32x4 Pown[3];
  STAMP(2);
#pragma unroll
  for (int c = 0; c < 8; ++c)
#pragma unroll
    for (int t = 0; t < 3; ++t) SA[c >> 2][t] = mfma(kf[c], qf[c & 3][t], (c & 3) == 0 ? zero4 : SA[c >> 2][t]);
  {
    float m[3];
    head_max(SA, m);
    static_for<0, 4>([&](auto ic) { constexpr int i = decltype(ic)::value; dma_nt<i * 1024>(voffK[i], kbase + 2 * 32768, w4k); });
#pragma unroll
    for (int t = 0; t < 3; ++t) {
      nm[t] = f32x4{-m[t], -m[t], -m[t], -m[t]};
      SA[0][t] += nm[t];
      SA[1][t] += nm[t];
    }
  }

  struct Req { const char* src; unsigned ex, x1, x2, x3, vb; bool shared; };
  auto req_issue = [&](auto ic, const Req& r) {
    constexpr int i = decltype(ic)::value;
    if constexpr (i == 0) dma_0<0>(r.vb, r.src, r.shared);
    else dma_0<i * 1024>(r.vb ^ (i == 1 ? r.x1 : (i == 2 ? r.x2 : r.x3)), r.src + i * r.ex, r.shared);
  };
  auto pv3 = [&](int dt, const u32x4& v, const u32x4 (&p)[3]) {
#pragma unroll
    for (int t = 0; t < 3; ++t) O[dt][t] = mfma(v, p[t], O[dt][t]);
  };
  auto xphase = [&](auto ksc, auto dsc, auto lastc, f32x4(&SC)[2][3], f32x4(&SN)[2][3], const Req& rq, int t) {
    constexpr unsigned ko = decltype(ksc)::value * SLOT;
    constexpr bool LAST = decltype(lastc)::value;
    LDMA(set_m0(w4k + decltype(dsc)::value * SLOT));
    auto qk = [&](auto cc) {
      constexpr int c = decltype(cc)::value, kt = c >> 2, s = c & 3;
      if constexpr (!LAST) {
#pragma unroll
        for (int tt = 0; tt < 3; ++tt) SN[kt][tt] = mfma(kf[c], qf[s][tt], s == 0 ? nm[tt] : SN[kt][tt]);
      }
    };
    if constexpr (!LAST) {
#pragma unroll
      for (int c = 0; c < 4; ++c) kf[c] = lds_ld128(ka[c] + ko);
    }
    SB();
    pv3(2, vg[2], Ppar);
    u32x2 a0 = p_unit(SC[0][0]);
    SB();
    pv3(3, vg[3], Ppar);
    u32x2 a1 = p_unit(SC[1][0]);
    Pown[0] = p_put(0, a0, a1);
    SB();
    if constexpr (!LAST) {
#pragma unroll
      for (int c = 4; c < 8; ++c) kf[c] = lds_ld128(ka[c & 3] + ko + 4096);
    }
    LDMA(req_issue(IC(0), rq));
    SB();
    qk(IC(0));
    a0 = p_unit(SC[0][1]);
    SB();
    qk(IC(1));
    a1 = p_unit(SC[1][1]);
    Pown[1] = p_put(1, a0, a1);
    ps[0] = mfma(ones, Pown[0], zero4);
    SB();
    LDMA(req_issue(IC(1), rq));
    SB();
    qk(IC(2));
    a0 = p_unit(SC[0][2]);
    SB();
    qk(IC(3));
    a1 = p_unit(SC[1][2]);
    Pown[2] = p_put(2, a0, a1);
    ps[1] = mfma(ones, Pown[1], zero4);
    SB();
    LDMA(req_issue(IC(2), rq));
    SB();
    qk(IC(4));
    ps[2] = mfma(ones, Pown[2], zero4);
    SB();
    LDMA(req_issue(IC(3), rq));
    SB();
    qk(IC(5));
    {
      const unsigned u0 = f2u(ps[0][0]), u1 = f2u(ps[1][0]), u2 = f2u(ps[2][0]);
      const unsigned um = max(max(u0, u1), u2);
      if (um > SUM_CAP) lds_stu(FL_OFF + t * 4, 1u);
    }
    SB();
  };
  auto yphase = [&](auto vsc, auto dsc, auto lastc, f32x4(&SC)[2][3], f32x4(&SN)[2][3], const Req& rq, int t) {
    constexpr unsigned vo = (decltype(vsc)::value - 2) * SLOT;
    constexpr bool LAST = decltype(lastc)::value;
    if constexpr (!LAST) LDMA(set_m0(w4k + decltype(dsc)::value * SLOT));
    const unsigned flagv = lds_ldu(FL_OFF + t * 4);
    SB();
    u32x4 vf[4];
#pragma unroll
    for (int dt = 0; dt < 4; ++dt) vf[dt] = v_frag(va[dt] + vo);
    SB();
    if constexpr (!LAST) {
#pragma unroll
      for (int tt = 0; tt < 3; ++tt) SN[1][tt] = mfma(kf[6], qf[2][tt], SN[1][tt]);
#pragma unroll
      for (int tt = 0; tt < 3; ++tt) SN[1][tt] = mfma(kf[7], qf[3][tt], SN[1][tt]);
    }
    SB();
    if (__builtin_amdgcn_readfirstlane(flagv)) {
      float m[3];
      head_max(SC, m);
#pragma unroll
      for (int tt = 0; tt < 3; ++tt) {
        const float d = fmaxf(m[tt], 0.f);
        const float alpha = ex2(-d);
        const f32x4 d4 = {d, d, d, d};
        lsum[tt] *= alpha;
#pragma unroll
        for (int dt = 0; dt < 4; ++dt) O[dt][tt] *= alpha;
        nm[tt] -= d4;
        SC[0][tt] -= d4;
        SC[1][tt] -= d4;
        if constexpr (!LAST) { SN[0][tt] -= d4; SN[1][tt] -= d4; }
        Pown[tt] = p_put(tt, p_unit(SC[0][tt]), p_unit(SC[1][tt]));
        ps[tt] = mfma(ones, Pown[tt], zero4);
      }
      lds_barrier();
    }
    SB();
#pragma unroll
    for (int tt = 0; tt < 3; ++tt) Ppar[tt] = lds_ld128((pw ^ 1024u) + tt * 2048);
#pragma unroll
    for (int dt = 0; dt < 4; ++dt) vg[dt] = v_frag((va[dt] ^ 8192u) + vo);
#pragma unroll
    for (int tt = 0; tt < 3; ++tt) lsum[tt] += ps[tt][0];
    SB();
    pv3(0, vf[0], Pown);
    if constexpr (!LAST) LDMA(req_issue(IC(0), rq));
    SB();
    pv3(1, vf[1], Pown);
    if constexpr (!LAST) LDMA(req_issue(IC(1), rq));
    SB();
    pv3(2, vf[2], Pown);
    if constexpr (!LAST) LDMA(req_issue(IC(2), rq));
    SB();
    pv3(3, vf[3], Pown);
    if constexpr (!LAST) LDMA(req_issue(IC(3), rq));
    SB();
    pv3(0, vg[0], Ppar);
    pv3(1, vg[1], Ppar);
    if constexpr (LAST) {
      pv3(2, vg[2], Ppar);
      pv3(3, vg[3], Ppar);
    }
    SB();
  };
  auto wsrc = [&](int wi) { return wobase + (size_t)(wi >> 1) * 128 * 1024 + (wi & 1) * 256; };
  #define LAUNDER(v) asm volatile("" : "+v"(v))
  auto reqV = [&](int t) { LAUNDER(vxb); return Req{vbase + (size_t)t * 32768, 0u, 0x80u, 0u, 0x80u, vxb, false}; };
  auto reqK = [&](int t) { LAUNDER(vyb); return Req{kbase + (size_t)t * 32768, 0u, 0xC0u, 0x80u, 0x40u, vyb, false}; };
  auto reqW = [&](int wi) { LAUNDER(wvb); return Req{wsrc(wi), 3072u, 0x80u, 0u, 0x80u, wvb, true}; };

#pragma unroll 1
  for (int t2 = 0; t2 < 6; t2 += 2) {
    wait_barrier<8>();
    STAMP(3 + 2 * t2);
    if (t2 == 0) do_touch();
    xphase(IC(1), IC(3), std::false_type{}, SA, SB_, reqV(t2 + 1), t2);
    DSTAMP(25);
    wait_barrier<8>();
    STAMP(4 + 2 * t2);
    yphase(IC(2), IC(1), std::false_type{}, SA, SB_, reqK(t2 + 3), t2);
    DSTAMP(26);
    wait_barrier<8>();
    STAMP(5 + 2 * t2);
    xphase(IC(0), IC(2), std::false_type{}, SB_, SA, reqV(t2 + 2), t2 + 1);
    Req ry = t2 == 4 ? reqW(0) : reqK(t2 + 4);
    ry.shared = false;
    DSTAMP(27);
    wait_barrier<8>();
    STAMP(6 + 2 * t2);
    yphase(IC(3), IC(0), std::false_type{}, SB_, SA, ry, t2 + 1);
    DSTAMP(28);
  }
  wait_barrier<8>();
  STAMP(15);
  xphase(IC(1), IC(3), std::false_type{}, SA, SB_, reqV(7), 6);
  wait_barrier<8>();
  STAMP(16);
  yphase(IC(2), IC(1), std::false_type{}, SA, SB_, reqW(1), 6);
  wait_barrier<8>();
  STAMP(17);
  const unsigned epoch_v = __hip_atomic_load(epoch_ptr, __ATOMIC_RELAXED, __HIP_MEMORY_SCOPE_AGENT);
  SB();
  xphase(IC(0), IC(2), std::true_type{}, SB_, SA, reqW(2), 7);
  wait_barrier<8>();
  STAMP(18);
  yphase(IC(3), IC(3), std::true_type{}, SB_, SA, reqW(3), 7);

#pragma unroll
  for (int t = 0; t < 3; ++t)
    if (lane < 16) lds_stf(LS_OFF + ((hh * 4 + jq) * 3 + t) * 64 + lane * 4, lsum[t]);
  wait_barrier<0>();
  const unsigned tag = (unsigned)__builtin_amdgcn_readfirstlane(epoch_v) + 1u;
  SB();
  STAMPR(19);
  set_m0(w4k + 3 * SLOT);
  static_for<0, 4>([&](auto ic) { req_issue(ic, reqW(3)); });
  const unsigned ow = PB + hh * 12288 + (2 * (jq & 1) + (jq >> 1)) * 3072 + lane * 16;
  const unsigned ox = PB + hh * 12288 + (2 * (jq & 1) + ((jq >> 1) ^ 1)) * 3072 + lane * 16;
  f32x4 Om[2][3];
  float lt[3];
  {
#pragma unroll
    for (int t = 0; t < 3; ++t) lds_st128(ow + t * 1024, __builtin_bit_cast(u32x4, O[2][t]));
#pragma unroll
    for (int t = 0; t < 3; ++t) {
      float s = 0.f;
#pragma unroll
      for (int j = 0; j < 4; ++j) s += lds_ldf(LS_OFF + ((hh * 4 + j) * 3 + t) * 64 + i16 * 4);
      lt[t] = s;
    }
    lds_barrier();
#pragma unroll
    for (int t = 0; t < 3; ++t) Om[0][t] = O[0][t] + __builtin_bit_cast(f32x4, lds_ld128(ox + t * 1024));
    SB();
#pragma unroll
    for (int t = 0; t < 3; ++t) lds_st128(ox + t * 1024, __builtin_bit_cast(u32x4, O[3][t]));
    lds_barrier();
#pragma unroll
    for (int t = 0; t < 3; ++t) Om[1][t] = O[1][t] + __builtin_bit_cast(f32x4, lds_ld128(ow + t * 1024));
  }

  STAMP(34);
  const __amdgpu_buffer_rsrc_t ws_rs = __builtin_amdgcn_make_buffer_rsrc((void*)ws, 0, WS_END, 0x00020000);
  const unsigned reg1 = WS_IN * (unsigned)(b + 1), reg2 = reg1 + WS_IN2;
  const unsigned lbid = (unsigned)bid & 31u;

  float lam[3];
  u32x4 mine[3];
#pragma unroll
  for (int t = 0; t < 3; ++t) {
    const float inv = __builtin_amdgcn_rcpf(lt[t]);
    lam[t] = __builtin_amdgcn_logf(lt[t]) - nm[t][0];
#pragma unroll
    for (int dt = 0; dt < 2; ++dt) Om[dt][t] *= inv;
    mine[t] = u32x4{pk_bf16(Om[0][t][0], Om[0][t][1]), pk_bf16(Om[0][t][2], Om[0][t][3]), pk_bf16(Om[1][t][0], Om[1][t][1]),
                    pk_bf16(Om[1][t][2], Om[1][t][3])};
  }
  {
    const float l0 = lam[0], l1 = lam[1], l2 = lam[2];
    const unsigned lw = __builtin_bit_cast(unsigned, (lane & 32) ? l2 : ((lane & 16) ? l1 : l0));
    unsigned h1, h2;
    hash13(mine[0], mine[1], mine[2], lw, tag, h1, h2);
    const unsigned dst = reg1 + (lbid ^ 16u) * 32768u + (unsigned)(w * 4096 + lane * 16);
    __builtin_amdgcn_raw_buffer_store_b128(mine[0], ws_rs, dst, 0, 16);
    __builtin_amdgcn_raw_buffer_store_b128(mine[1], ws_rs, dst + 1024, 0, 16);
    __builtin_amdgcn_raw_buffer_store_b128(mine[2], ws_rs, dst + 2048, 0, 16);
    __builtin_amdgcn_raw_buffer_store_b128(u32x4{lw, h1, h2, tag}, ws_rs, dst + 3072, 0, 16);
    SB();
    if (lane == 0) __builtin_amdgcn_raw_buffer_store_b32(tag, ws_rs, WS_HINT1 + (unsigned)((bid ^ 16) * 128 + w * 4), 0, 16);
  }
  STAMP(35);
  unsigned wa_[2];
  {
    const int q_ = (lane >> 2) & 3, p = lane & 3;
#pragma unroll
    for (int dt = 0; dt < 2; ++dt)
      wa_[dt] = hh * SLOT + (4 * g + q_) * 256 + (((4 * jq + 2 * dt + (p >> 1)) ^ (2 * ((4 * g + q_) & 7))) << 4) + 8 * (p & 1);
  }
  u32x4 wf[8][2];
#pragma unroll
  for (int st = 0; st < 4; ++st)
#pragma unroll
    for (int dt = 0; dt < 2; ++dt) wf[st][dt] = v_frag(wa_[dt] + st * 8192);
  SB();
  u32x4 th[4];
  STAMP(36);
  recv4<1024, 37, HOP1_AUX>(ws_rs, WS_HINT1 + (unsigned)(bid * 128 + w * 4), reg1 + lbid * 32768u + (unsigned)(w * 4096 + lane * 16), tag, th, lane, w);
  STAMPR(20);
  {
    const unsigned u = th[3][0];
    auto a = __builtin_amdgcn_permlane32_swap(u, u, false, false);
    auto c0 = __builtin_amdgcn_permlane16_swap(a[0], a[0], false, false);
    auto c1 = __builtin_amdgcn_permlane16_swap(a[1], a[1], false, false);
    const float lb[3] = {u2f(c0[0]), u2f(c0[1]), u2f(c1[0])};
#pragma unroll
    for (int t = 0; t < 3; ++t) {
      const float mx = fmaxf(lam[t], lb[t]);
      float wa = ex2(lam[t] - mx), wb = ex2(lb[t] - mx);
      const float inv = __builtin_amdgcn_rcpf(wa + wb);
      wa *= inv;
      wb *= inv;
      float m[8];
#pragma unroll
      for (int k = 0; k < 4; ++k) {
        m[2 * k] = wa * Om[k >> 1][t][2 * (k & 1)] + wb * bf_lo(th[t][k]);
        m[2 * k + 1] = wa * Om[k >> 1][t][2 * (k & 1) + 1] + wb * bf_hi(th[t][k]);
      }
      lds_st128(ow + t * 1024, u32x4{pk_bf16(m[0], m[1]), pk_bf16(m[2], m[3]), pk_bf16(m[4], m[5]), pk_bf16(m[6], m[7])});
    }
  }
  wait_barrier<0>();
  STAMP(21);

  const unsigned pr = PB + lane * 16;
  f32x4 A[2][3];
#pragma unroll
  for (int dt = 0; dt < 2; ++dt)
#pragma unroll
    for (int t = 0; t < 3; ++t) A[dt][t] = zero4;
  {
    u32x4 pf[8][3];
#pragma unroll
    for (int st = 0; st < 4; ++st)
#pragma unroll
      for (int t = 0; t < 3; ++t) pf[st][t] = lds_ld128(pr + st * 3072 + t * 1024);
#pragma unroll
    for (int st = 4; st < 8; ++st) {
#pragma unroll
      for (int dt = 0; dt < 2; ++dt) wf[st][dt] = v_frag(wa_[dt] + 2 * SLOT + (st - 4) * 8192);
#pragma unroll
      for (int t = 0; t < 3; ++t) pf[st][t] = lds_ld128(pr + st * 3072 + t * 1024);
    }
    SB();
#pragma unroll
    for (int st = 0; st < 8; ++st)
#pragma unroll
      for (int dt = 0; dt < 2; ++dt)
#pragma unroll
        for (int t = 0; t < 3; ++t) A[dt][t] = mfma(wf[st][dt], pf[st][t], A[dt][t]);
  }
  STAMP(22);

  {
    u32x4 f[3];
#pragma unroll
    for (int t = 0; t < 3; ++t)
      f[t] = u32x4{pk_bf16(A[0][t][0], A[0][t][1]), pk_bf16(A[0][t][2], A[0][t][3]), pk_bf16(A[1][t][0], A[1][t][1]),
                   pk_bf16(A[1][t][2], A[1][t][3])};
    unsigned h1, h2;
    hash13(f[0], f[1], f[2], 0u, tag, h1, h2);
    const int rho = 2 * w + (i16 >> 3);
    const int rbid = (rho >> 1) + 8 * ((rho & 1) + 2 * kh + 4 * b);
    const int pidx = kvh * 2 + hp;
    const unsigned dst = reg2 + (unsigned)((rbid & 31) * 32768 + pidx * 2048 + (8 * g + (i16 & 7)) * 16);
    __builtin_amdgcn_raw_buffer_store_b128(f[0], ws_rs, dst, 0, 16);
    __builtin_amdgcn_raw_buffer_store_b128(f[1], ws_rs, dst + 512, 0, 16);
    __builtin_amdgcn_raw_buffer_store_b128(f[2], ws_rs, dst + 1024, 0, 16);
    __builtin_amdgcn_raw_buffer_store_b128(u32x4{0u, h1, h2, tag}, ws_rs, dst + 1536, 0, 16);
    SB();
    if ((lane & 55) == 0) __builtin_amdgcn_raw_buffer_store_b32(tag, ws_rs, WS_HINT2 + (unsigned)(rbid * 128 + pidx * 4), 0, 16);
  }
  {
    STAMP(39);
    recv4<512, 40, HOP2_AUX>(ws_rs, WS_HINT2 + (unsigned)(bid * 128 + (tid >> 5) * 4), reg2 + lbid * 32768u + (unsigned)((tid >> 5) * 2048 + (tid & 31) * 16), tag, th, lane, w);
    STAMPR(23);
#pragma unroll
    for (int t = 0; t < 3; ++t) {
      f32x4 r;
#pragma unroll
      for (int k = 0; k < 2; ++k) {
        const float a0 = half_sum(bf_lo(th[t][k])), a1 = half_sum(bf_hi(th[t][k]));
        const float b0 = half_sum(bf_lo(th[t][2 + k])), b1 = half_sum(bf_hi(th[t][2 + k]));
        r[2 * k] = lane < 32 ? a0 : b0;
        r[2 * k + 1] = lane < 32 ? a1 : b1;
      }
      lds_st128(w * 3072 + t * 1024 + lane * 16, __builtin_bit_cast(u32x4, r));
    }
  }
  lds_barrier();
  if (tid < 192) {
    const int t = tid >> 6;
    f32x4 s = __builtin_bit_cast(f32x4, lds_ld128(t * 1024 + lane * 16));
#pragma unroll
    for (int v = 1; v < 8; ++v) s += __builtin_bit_cast(f32x4, lds_ld128(v * 3072 + t * 1024 + lane * 16));
    const int rho = xk * 2 + hp;
    const int nt = lane >> 5, l32 = lane & 31, gg = l32 >> 3, q = 8 * (rho & 1) + (l32 & 7);
    const int col = 256 * kh + 32 * (rho >> 1) + 16 * nt + 4 * gg;
    u32x2 o = {pk_bf16(s[0], s[1]), pk_bf16(s[2], s[3])};
    *(u32x2*)(out + ((size_t)b * SQ + 16 * t + q) * DM + col) = o;
  }
  STAMPR(24);
  STAMP_DUMP();
  if (tid == 0 && xk == 0 && hp == 0 && kh == 0) __hip_atomic_store(epoch_ptr, tag, __ATOMIC_RELAXED, __HIP_MEMORY_SCOPE_AGENT);
}

}

extern "C" void kernel_launch(void* const* d_in, const int* in_sizes, int n_in, void* d_out, int out_size, void* d_ws,
                              size_t ws_size, hipStream_t stream) {
  (void)in_sizes; (void)n_in; (void)out_size; (void)ws_size;
  (void)hipFuncSetAttribute((const void*)gqa_fused_kernel, hipFuncAttributeMaxDynamicSharedMemorySize, LDS_BYTES);
  gqa_fused_kernel<<<256, 512, LDS_BYTES, stream>>>((const uint16_t*)d_in[0], (const uint16_t*)d_in[1], (const uint16_t*)d_in[2],
                                                    (const uint16_t*)d_in[3], (const uint16_t*)d_in[4], (uint16_t*)d_out,
                                                    (unsigned char*)d_ws);
}
```

```cpp
#include <hip/hip_runtime.h>
#include <hip/hip_bf16.h>
#include <stdint.h>
#include <type_traits>

#define AS3 __attribute__((address_space(3)))
typedef __attribute__((ext_vector_type(8))) __bf16 bf16x8;
typedef __attribute__((ext_vector_type(2))) __bf16 bf16x2;
typedef __attribute__((ext_vector_type(4))) float f32x4;
typedef __attribute__((ext_vector_type(2))) float f32x2;
typedef __attribute__((ext_vector_type(4))) unsigned u32x4;
typedef __attribute__((ext_vector_type(2))) unsigned u32x2;
typedef __attribute__((ext_vector_type(4))) short i16x4;

#define STAMP(i) do {} while (0)
#define STAMPR(i) do {} while (0)
#define STAMP_DUMP() do { __builtin_amdgcn_s_waitcnt(0xc07f);} while (0)
#define SB() __builtin_amdgcn_sched_barrier(0)
#define IC(v) std::integral_constant<int, (v)>{}
#define HOP1_AUX 0
#define HOP2_AUX 0
#define NPASS 1
#define MAPK 0
#define TOUCH_AT 1
#define KVPOL " nt"
#define LDMA(x) x
#define DSTAMP(i) do {} while (0)

namespace {

constexpr int SQ = 48, DM = 512, SK = 2048, DH = 128, NQ = 4096;

constexpr unsigned SLOT = 32768;
constexpr unsigned PB = 131072;
constexpr unsigned MISC = 155648;
constexpr unsigned MX_OFF = MISC;
constexpr unsigned LS_OFF = MISC + 1536;
constexpr unsigned FL_OFF = MISC + 3072;
constexpr unsigned TOUCH_OFF = MISC + 3328;
constexpr unsigned XIMG = 98304;
constexpr unsigned LDS_BYTES = 163840;

constexpr unsigned WS_EPOCH = 0;
constexpr unsigned WS_HINT1 = 4096;
constexpr unsigned WS_HINT2 = WS_HINT1 + 32768;
constexpr unsigned WS_IN = 2u << 20;
constexpr unsigned WS_IN2 = 1u << 20;
constexpr unsigned WS_END = WS_IN * 9;

constexpr float SCL = 0.08838834764831845f * 1.4426950408889634f;
constexpr unsigned SUM_CAP = 0x47800000u;
constexpr unsigned SPIN_CAP = 1u << 16;

__device__ __forceinline__ u32x4 lds_ld128(unsigned a) { return *(const AS3 u32x4*)(size_t)a; }
__device__ __forceinline__ void lds_st128(unsigned a, u32x4 v) { *(AS3 u32x4*)(size_t)a = v; }
__device__ __forceinline__ float lds_ldf(unsigned a) { return *(const AS3 float*)(size_t)a; }
__device__ __forceinline__ void lds_stf(unsigned a, float v) { *(AS3 float*)(size_t)a = v; }
__device__ __forceinline__ unsigned lds_ldu(unsigned a) { return *(const AS3 unsigned*)(size_t)a; }
__device__ __forceinline__ void lds_stu(unsigned a, unsigned v) { *(AS3 unsigned*)(size_t)a = v; }
__device__ __forceinline__ u32x2 lds_tr(unsigned a) {
  i16x4 r = __builtin_amdgcn_ds_read_tr16_b64_v4i16((AS3 i16x4*)(size_t)a);
  return __builtin_bit_cast(u32x2, r);
}
__device__ __forceinline__ f32x4 mfma(u32x4 a, u32x4 b, f32x4 c) {
  return __builtin_amdgcn_mfma_f32_16x16x32_bf16(__builtin_bit_cast(bf16x8, a), __builtin_bit_cast(bf16x8, b), c, 0, 0, 0);
}
__device__ __forceinline__ unsigned pk_bf16(float lo, float hi) {
  f32x2 v = {lo, hi};
  bf16x2 r = __builtin_convertvector(v, bf16x2);
  return __builtin_bit_cast(unsigned, r);
}
__device__ __forceinline__ float u2f(unsigned u) { return __builtin_bit_cast(float, u); }
__device__ __forceinline__ unsigned f2u(float f) { return __builtin_bit_cast(unsigned, f); }
__device__ __forceinline__ float bf_lo(unsigned u) { return __builtin_bit_cast(float, u << 16); }
__device__ __forceinline__ float bf_hi(unsigned u) { return __builtin_bit_cast(float, u & 0xffff0000u); }
__device__ __forceinline__ float ex2(float v) { return __builtin_amdgcn_exp2f(v); }

template <int OFF>
__device__ __forceinline__ void dma_nt(unsigned voff, const void* sbase, unsigned m0v) {
  asm volatile("s_mov_b32 m0, %2\n\ts_nop 0\n\tglobal_load_lds_dwordx4 %0, %1 offset:%3" KVPOL
               :: "v"(voff), "s"(sbase), "s"(m0v), "n"(OFF) : "memory");
}
template <int OFF>
__device__ __forceinline__ void dma_def(unsigned voff, const void* sbase, unsigned m0v) {
  asm volatile("s_mov_b32 m0, %2\n\ts_nop 0\n\tglobal_load_lds_dwordx4 %0, %1 offset:%3"
               :: "v"(voff), "s"(sbase), "s"(m0v), "n"(OFF) : "memory");
}
__device__ __forceinline__ void set_m0(unsigned m0v) { asm volatile("s_mov_b32 m0, %0\n\ts_nop 0" ::"s"(m0v) : "memory"); }
template <int OFF>
__device__ __forceinline__ void dma_0(unsigned vb, const void* sbase, bool shared) {
  if (shared) asm volatile("global_load_lds_dwordx4 %0, %1 offset:%2" ::"v"(vb), "s"(sbase), "n"(OFF) : "memory");
  else asm volatile("global_load_lds_dwordx4 %0, %1 offset:%2" KVPOL ::"v"(vb), "s"(sbase), "n"(OFF) : "memory");
}
__device__ __forceinline__ void touch(unsigned voff, const void* sbase, unsigned m0v) {
  asm volatile("s_mov_b32 m0, %2\n\ts_nop 0\n\tglobal_load_lds_dword %0, %1 sc1" ::"v"(voff), "s"(sbase), "s"(m0v) : "memory");
}
template <int I, int N, class F>
__device__ __forceinline__ void static_for(F&& f) {
  if constexpr (I < N) {
    f(std::integral_constant<int, I>{});
    static_for<I + 1, N>(f);
  }
}
template <int N>
__device__ __forceinline__ void wait_barrier() {
  asm volatile("s_waitcnt vmcnt(%0) lgkmcnt(0)\n\ts_barrier" ::"n"(N) : "memory");
}
__device__ __forceinline__ void lds_barrier() { asm volatile("s_waitcnt lgkmcnt(0)\n\ts_barrier" ::: "memory"); }

__device__ __forceinline__ float grp_max(float v) {
  unsigned u = __builtin_bit_cast(unsigned, v);
  auto a = __builtin_amdgcn_permlane16_swap(u, u, false, false);
  v = fmaxf(u2f(a[0]), u2f(a[1]));
  u = __builtin_bit_cast(unsigned, v);
  auto c = __builtin_amdgcn_permlane32_swap(u, u, false, false);
  return fmaxf(u2f(c[0]), u2f(c[1]));
}
__device__ __forceinline__ float half_sum(float v) {
  unsigned u = __builtin_bit_cast(unsigned, v);
  auto c = __builtin_amdgcn_permlane32_swap(u, u, false, false);
  return u2f(c[0]) + u2f(c[1]);
}

template <int R>
__device__ __forceinline__ unsigned rot(unsigned h) { return __builtin_amdgcn_alignbit(h, h, R); }
__device__ __forceinline__ void hash13(const u32x4& a, const u32x4& b, const u32x4& c, unsigned d, unsigned tag,
                                       unsigned& h1, unsigned& h2) {
  auto x3 = [](unsigned p, unsigned q, unsigned r) { return __builtin_amdgcn_bitop3_b32(p, q, r, 0x96); };
  const unsigned x0 = x3(a[0], rot<5>(a[1]), rot<10>(a[2])), x1 = x3(rot<15>(a[3]), rot<20>(b[0]), rot<25>(b[1]));
  const unsigned x2 = x3(rot<30>(b[2]), rot<3>(b[3]), rot<8>(c[0])), x4 = x3(rot<13>(c[1]), rot<18>(c[2]), rot<23>(c[3]));
  h1 = x3(x3(x0, x1, x2), x4, rot<28>(d) ^ (tag * 0x9E3779B1u + 0x7F4A7C15u));
  h2 = (a[0] + a[1] + a[2]) + (a[3] + b[0] + b[1]) + (b[2] + b[3] + c[0]) + (c[1] + c[2] + c[3]) + (d + 2u * tag + 1u);
}

template <int STR, int SI, int MODE>
__device__ __forceinline__ void recv4(const __amdgpu_buffer_rsrc_t& rs, unsigned hint, unsigned src, unsigned tag, u32x4 (&th)[4], int lane, int w) {
  unsigned n1 = 0, n2 = 0;
  auto attempt = [&]() {
    asm volatile("" ::: "memory");
#pragma unroll
    for (int k = 0; k < 4; ++k) th[k] = __builtin_amdgcn_raw_buffer_load_b128(rs, src + k * STR, 0, 16);
    unsigned h1, h2;
    hash13(th[0], th[1], th[2], th[3][0], tag, h1, h2);
    const bool ok = (h1 == th[3][1]) & (h2 == th[3][2]);
    ++n2;
    return __builtin_amdgcn_ballot_w64(ok) == ~0ull;
  };
  bool done = false;
  if constexpr (MODE & 1) done = attempt();
  if (!done) {
    if constexpr (!(MODE & 2)) {
#pragma unroll 1
      for (unsigned s2 = 0; s2 < SPIN_CAP; ++s2) {
        asm volatile("" ::: "memory");
        const unsigned tg = __builtin_amdgcn_raw_buffer_load_b32(rs, hint, 0, 16);
        ++n1;
        if (__builtin_amdgcn_ballot_w64(tg == tag) == ~0ull) break;
        __builtin_amdgcn_s_sleep(1);
      }
    }
    STAMP(SI);
#pragma unroll 1
    for (unsigned spin = 0; spin < SPIN_CAP; ++spin) {
      if (attempt()) break;
    }
  } else {
    STAMP(SI);
  }
  (void)n1; (void)n2; (void)lane; (void)w;
}

__global__ __launch_bounds__(512, 2) void gqa_fused_kernel(const uint16_t* __restrict__ x, const uint16_t* __restrict__ kc,
                                                           const uint16_t* __restrict__ vc, const uint16_t* __restrict__ wq,
                                                           const uint16_t* __restrict__ wo, uint16_t* __restrict__ out,
                                                           unsigned char* __restrict__ ws) {
#pragma unroll 1
  for (int pass = 0; pass < NPASS; ++pass) {
  __syncthreads();
  int tid = threadIdx.x;
  asm volatile("" : "+v"(tid));
  const int lane = tid & 63;
  const int w = __builtin_amdgcn_readfirstlane(tid >> 6);
  int bid = blockIdx.x;
  asm volatile("" : "+s"(bid));
  const int xk = bid & 7, slot = bid >> 3, hp = slot & 1, kh = (slot >> 1) & 1, b = slot >> 2;
  const int kvh = xk;
  const int g = lane >> 4, i16 = lane & 15;
  const int hh = w >> 2, jq = w & 3;
  const int head0 = kvh * 4 + hp * 2;

  STAMPR(0);
  unsigned* epoch_ptr = (unsigned*)(ws + WS_EPOCH + (size_t)b * 256);

  const char* kbase = (const char*)kc + ((size_t)(b * 8 + kvh) * SK + kh * 1024) * (DH * 2);
  const char* vbase = (const char*)vc + ((size_t)(b * 8 + kvh) * SK + kh * 1024) * (DH * 2);
  const char* wobase = (const char*)wo + ((size_t)head0 * DH * DM + kh * 256) * 2;

  unsigned voffK[4], voffV[2];
#pragma unroll
  for (int i = 0; i < 4; ++i) voffK[i] = (unsigned)(w * 4096 + g * 256 + ((i16 ^ (4 * i + g) ^ ((i & 1) << 3)) << 4));
#pragma unroll
  for (int i = 0; i < 2; ++i) voffV[i] = (unsigned)(w * 4096 + g * 256 + ((i16 ^ (2 * (4 * i + g))) << 4));
  const unsigned w4k = (unsigned)(w * 4096);
  u32x4 qf[4][3], kf[8];
  f32x4 O[4][3];
  float lsum[3];
  u32x4 Ppar[3], vg[4];
  unsigned ka[4];
#pragma unroll
  for (int s = 0; s < 4; ++s) ka[s] = (32 * jq + i16) * 256 + (((4 * g + s) ^ i16 ^ ((i16 & 4) << 1)) << 4);

  auto do_touch = [&]() {
    if (w == 0) touch((lane & 1) ? ((lane & 2) ? WS_HINT2 : WS_HINT1) + (unsigned)bid * 128u
                                 : WS_IN * (unsigned)(b + 1) + (unsigned)(lane >> 1) * 65536u, ws, TOUCH_OFF);
  };
  {
    STAMP(42);
    const char* xb = (const char*)x + (size_t)b * SQ * DM * 2;
    const int rot = (slot >> 1) & 7;
    const char* wqb = (const char*)wq + head0 * (DH * 2);
    const unsigned voffQ = (unsigned)((8 * w + (lane >> 5)) * (NQ * 2) + (((lane & 31) ^ (2 * ((lane >> 5) + 4 * (w & 1)))) << 4));
    const int xrow = 6 * w + (lane >> 3);
    const unsigned voffX = (unsigned)(xrow * 1024 + (((lane & 7) ^ ((xrow >> 1) & 7)) << 4));
    auto rq_piece = [&](auto sc, auto pc) {
      constexpr int st = decltype(sc)::value, i = decltype(pc)::value - 1;
      const int tt = (st + rot) & 7;
      if constexpr (i < 0) {
        if (lane < 48) dma_def<0>(voffX, xb + tt * 128, (unsigned)(PB + (st & 3) * 6144 + w * 768));
      } else {
        const char* src = wqb + (size_t)(tt * 64) * (NQ * 2);
        dma_def<i * 1024>(voffQ ^ ((i & 1) * 64u), src + i * (2 * NQ * 2 - 1024), w4k + (st & 3) * SLOT);
      }
    };
    auto rq_all = [&](auto sc) { static_for<0, 5>([&](auto pc) { rq_piece(sc, pc); }); };
    rq_all(IC(0));
    rq_all(IC(1));
    SB();
    STAMP(43);
    SB();
    if (tid < 16) lds_stu(FL_OFF + tid * 4, 0u);
#pragma unroll
    for (int t = 0; t < 3; ++t) {
#pragma unroll
      for (int dt = 0; dt < 4; ++dt) {
        O[dt][t] = f32x4{0.f, 0.f, 0.f, 0.f};
        asm volatile("" : "+v"(O[dt][t]));
      }
      lsum[t] = 0.f;
      Ppar[t] = u32x4{0u, 0u, 0u, 0u};
      asm volatile("" : "+v"(lsum[t]), "+v"(Ppar[t]));
    }
    vg[2] = u32x4{0u, 0u, 0u, 0u};
    vg[3] = u32x4{0u, 0u, 0u, 0u};
    asm volatile("" : "+v"(vg[2]), "+v"(vg[3]));
    unsigned aq[2];
    {
      const int q_ = (lane >> 2) & 3, p = lane & 3;
#pragma unroll
      for (int dt = 0; dt < 2; ++dt)
        aq[dt] = (8 * g + q_) * 512 + (((4 * w + 2 * dt + (p >> 1)) ^ (2 * (q_ + 4 * (g & 1)))) << 4) + 8 * (p & 1);
    }
    const unsigned xl = PB + i16 * 128 + ((g ^ (i16 >> 1)) << 4);
    f32x4 acc[2][3];
#pragma unroll
    for (int dt = 0; dt < 2; ++dt)
#pragma unroll
      for (int t = 0; t < 3; ++t) acc[dt][t] = f32x4{0.f, 0.f, 0.f, 0.f};
    u32x4 af[2][2][2], bf[2][2][3];
    auto frag_a = [&](auto sc, int e) {
      constexpr int st = decltype(sc)::value, pp = st & 1;
      constexpr unsigned so = (st & 3) * SLOT;
#pragma unroll
      for (int dt = 0; dt < 2; ++dt) {
        const u32x2 v0 = lds_tr(aq[dt] + so + e * 16384), v1 = lds_tr(aq[dt] + so + e * 16384 + 2048);
        af[pp][e][dt] = u32x4{v0[0], v0[1], v1[0], v1[1]};
      }
    };
    auto frag_b = [&](auto sc, int e) {
      constexpr int st = decltype(sc)::value, pp = st & 1;
      constexpr unsigned xo = (st & 3) * 6144;
#pragma unroll
      for (int t = 0; t < 3; ++t) bf[pp][e][t] = lds_ld128((xl ^ (e * 64u)) + xo + t * 2048);
    };
    auto frags = [&](auto sc) { frag_a(sc, 0); frag_b(sc, 0); frag_a(sc, 1); frag_b(sc, 1); };
    auto qm = [&](int pp, int e, int dt) {
#pragma unroll
      for (int t = 0; t < 3; ++t) acc[dt][t] = mfma(af[pp][e][dt], bf[pp][e][t], acc[dt][t]);
    };
    wait_barrier<5>();
    STAMP(29);
    rq_all(IC(2));
    SB();
    frags(IC(0));
    SB();
    rq_all(IC(3));
    SB();
    static_for<0, 7>([&](auto sc) {
      constexpr int st = decltype(sc)::value, pp = st & 1;
      if constexpr (st <= 4) wait_barrier<10>(); else if constexpr (st == 5) wait_barrier<9>(); else wait_barrier<8>();
      SB();
      STAMP(44 + st);
      auto piece = [&](auto pc) {
        constexpr int p = decltype(pc)::value;
        if constexpr (st <= 3) rq_piece(IC(st + 4), pc);
        else if constexpr (p >= 1) {
          if constexpr (st == 4) dma_nt<(p - 1) * 1024>(voffK[p - 1], kbase, w4k);
          if constexpr (st == 5) dma_nt<(p - 1) * 1024>(voffK[p - 1], kbase + 32768, w4k + SLOT);
          if constexpr (st == 6) dma_nt<(p - 1) * 1024>(voffV[(p - 1) & 1], vbase, w4k + 2 * SLOT);
        }
        SB();
      };
      piece(IC(0));
      piece(IC(1));
      frag_a(IC(st + 1), 0); qm(pp, 0, 0); SB();
      piece(IC(2));
      frag_b(IC(st + 1), 0); qm(pp, 0, 1); SB();
      piece(IC(3));
      frag_a(IC(st + 1), 1); qm(pp, 1, 0); SB();
      piece(IC(4));
      frag_b(IC(st + 1), 1); qm(pp, 1, 1); SB();
    });
    STAMP(30);
    wait_barrier<8>();
#pragma unroll
    for (int c = 0; c < 8; ++c) kf[c] = lds_ld128(ka[c & 3] + (c >> 2) * 4096);
    SB();
    qm(1, 0, 0); qm(1, 0, 1); qm(1, 1, 0); qm(1, 1, 1);
#pragma unroll
    for (int dt = 0; dt < 2; ++dt)
#pragma unroll
      for (int t = 0; t < 3; ++t) {
        const u32x2 v = {pk_bf16(acc[dt][t][0] * SCL, acc[dt][t][1] * SCL), pk_bf16(acc[dt][t][2] * SCL, acc[dt][t][3] * SCL)};
        *(AS3 u32x2*)(size_t)(XIMG + hh * 12288 + t * 4096 + (4 * jq + 2 * dt + (g >> 1)) * 256 + i16 * 16 + (g & 1) * 8) = v;
      }
    STAMP(31);
    lds_barrier();
#pragma unroll
    for (int s = 0; s < 4; ++s)
#pragma unroll
      for (int t = 0; t < 3; ++t) qf[s][t] = lds_ld128(XIMG + hh * 12288 + t * 4096 + (4 * g + s) * 256 + i16 * 16);
  }
  STAMPR(1);

  unsigned va[4];
  {
    const int q_ = (lane >> 2) & 3, p = lane & 3;
#pragma unroll
    for (int dt = 0; dt < 4; ++dt)
      va[dt] = 2 * SLOT + (32 * jq + 4 * g + q_) * 256 + (((8 * (jq & 1) + 2 * (dt ^ (jq & 2)) + (p >> 1)) ^ (2 * ((4 * g + q_) & 7))) << 4) + 8 * (p & 1);
  }
  const unsigned pw = PB + hh * 12288 + (jq >> 1) * 6144 + (jq & 1) * 1024 + lane * 16;

  f32x4 nm[3];
  f32x4 ps[3];
  const u32x4 ones = {0x3F803F80u, 0x3F803F80u, 0x3F803F80u, 0x3F803F80u};
  const f32x4 zero4 = {0.f, 0.f, 0.f, 0.f};

  auto tile_max = [&](const f32x4(&S)[2][3], int t) {
    float m = fmaxf(fmaxf(S[0][t][0], S[0][t][1]), S[0][t][2]);
    m = fmaxf(fmaxf(m, S[0][t][3]), S[1][t][0]);
    m = fmaxf(fmaxf(m, S[1][t][1]), S[1][t][2]);
    return fmaxf(m, S[1][t][3]);
  };
  auto head_max = [&](const f32x4(&S)[2][3], float (&m)[3]) {
#pragma unroll
    for (int t = 0; t < 3; ++t) {
      const float v = grp_max(tile_max(S, t));
      if (lane < 16) lds_stf(MX_OFF + ((hh * 4 + jq) * 3 + t) * 64 + lane * 4, v);
    }
    lds_barrier();
#pragma unroll
    for (int t = 0; t < 3; ++t) {
      float v = lds_ldf(MX_OFF + ((hh * 4 + 0) * 3 + t) * 64 + i16 * 4);
#pragma unroll
      for (int j = 1; j < 4; ++j) v = fmaxf(v, lds_ldf(MX_OFF + ((hh * 4 + j) * 3 + t) * 64 + i16 * 4));
      m[t] = v;
    }
  };
  auto p_unit = [&](const f32x4& s) { return u32x2{pk_bf16(ex2(s[0]), ex2(s[1])), pk_bf16(ex2(s[2]), ex2(s[3]))}; };
  auto p_put = [&](int t, u32x2 a, u32x2 c) {
    const u32x4 f = {a[0], a[1], c[0], c[1]};
    lds_st128(pw + t * 2048, f);
    return f;
  };
  auto v_frag = [&](unsigned a) {
    const u32x2 v0 = lds_tr(a), v1 = lds_tr(a + 4096);
    return u32x4{v0[0], v0[1], v1[0], v1[1]};
  };

  unsigned vxb = voffV[0], vyb = voffK[0];
  unsigned wvb = (unsigned)((16 * w + g) * 1024 + ((i16 ^ (2 * g)) << 4));

  f32x4 SA[2][3], SB_[2][3];
  u32x4 Pown[3];
  STAMP(2);
#pragma unroll
  for (int c = 0; c < 8; ++c)
#pragma unroll
    for (int t = 0; t < 3; ++t) SA[c >> 2][t] = mfma(kf[c], qf[c & 3][t], (c & 3) == 0 ? zero4 : SA[c >> 2][t]);
  {
    float m[3];
    head_max(SA, m);
    static_for<0, 4>([&](auto ic) { constexpr int i = decltype(ic)::value; dma_nt<i * 1024>(voffK[i], kbase + 2 * 32768, w4k); });
#pragma unroll
    for (int t = 0; t < 3; ++t) {
      nm[t] = f32x4{-m[t], -m[t], -m[t], -m[t]};
      SA[0][t] += nm[t];
      SA[1][t] += nm[t];
    }
  }

  struct Req { const char* src; unsigned ex, x1, x2, x3, vb; bool shared; };
  auto req_issue = [&](auto ic, const Req& r) {
    constexpr int i = decltype(ic)::value;
    if constexpr (i == 0) dma_0<0>(r.vb, r.src, r.shared);
    else dma_0<i * 1024>(r.vb ^ (i == 1 ? r.x1 : (i == 2 ? r.x2 : r.x3)), r.src + i * r.ex, r.shared);
  };
  auto pv3 = [&](int dt, const u32x4& v, const u32x4 (&p)[3]) {
#pragma unroll
    for (int t = 0; t < 3; ++t) O[dt][t] = mfma(v, p[t], O[dt][t]);
  };
  auto xphase = [&](auto ksc, auto dsc, auto lastc, f32x4(&SC)[2][3], f32x4(&SN)[2][3], const Req& rq, int t) {
    constexpr unsigned ko = decltype(ksc)::value * SLOT;
    constexpr bool LAST = decltype(lastc)::value;
    LDMA(set_m0(w4k + decltype(dsc)::value * SLOT));
    auto qk = [&](auto cc) {
      constexpr int c = decltype(cc)::value, kt = c >> 2, s = c & 3;
      if constexpr (!LAST) {
#pragma unroll
        for (int tt = 0; tt < 3; ++tt) SN[kt][tt] = mfma(kf[c], qf[s][tt], s == 0 ? nm[tt] : SN[kt][tt]);
      }
    };
    if constexpr (!LAST) {
#pragma unroll
      for (int c = 0; c < 4; ++c) kf[c] = lds_ld128(ka[c] + ko);
    }
    SB();
    pv3(2, vg[2], Ppar);
    u32x2 a0 = p_unit(SC[0][0]);
    SB();
    pv3(3, vg[3], Ppar);
    u32x2 a1 = p_unit(SC[1][0]);
    Pown[0] = p_put(0, a0, a1);
    SB();
    if constexpr (!LAST) {
#pragma unroll
      for (int c = 4; c < 8; ++c) kf[c] = lds_ld128(ka[c & 3] + ko + 4096);
    }
    LDMA(req_issue(IC(0), rq));
    SB();
    qk(IC(0));
    a0 = p_unit(SC[0][1]);
    SB();
    qk(IC(1));
    a1 = p_unit(SC[1][1]);
    Pown[1] = p_put(1, a0, a1);
    ps[0] = mfma(ones, Pown[0], zero4);
    SB();
    LDMA(req_issue(IC(1), rq));
    SB();
    qk(IC(2));
    a0 = p_unit(SC[0][2]);
    SB();
    qk(IC(3));
    a1 = p_unit(SC[1][2]);
    Pown[2] = p_put(2, a0, a1);
    ps[1] = mfma(ones, Pown[1], zero4);
    SB();
    LDMA(req_issue(IC(2), rq));
    SB();
    qk(IC(4));
    ps[2] = mfma(ones, Pown[2], zero4);
    SB();
    LDMA(req_issue(IC(3), rq));
    SB();
    qk(IC(5));
    {
      const unsigned u0 = f2u(ps[0][0]), u1 = f2u(ps[1][0]), u2 = f2u(ps[2][0]);
      const unsigned um = max(max(u0, u1), u2);
      if (um > SUM_CAP) lds_stu(FL_OFF + t * 4, 1u);
    }
    SB();
  };
  auto yphase = [&](auto vsc, auto dsc, auto lastc, f32x4(&SC)[2][3], f32x4(&SN)[2][3], const Req& rq, int t) {
    constexpr unsigned vo = (decltype(vsc)::value - 2) * SLOT;
    constexpr bool LAST = decltype(lastc)::value;
    if constexpr (!LAST) LDMA(set_m0(w4k + decltype(dsc)::value * SLOT));
    const unsigned flagv = lds_ldu(FL_OFF + t * 4);
    SB();
    u32x4 vf[4];
#pragma unroll
    for (int dt = 0; dt < 4; ++dt) vf[dt] = v_frag(va[dt] + vo);
    SB();
    if constexpr (!LAST) {
#pragma unroll
      for (int tt = 0; tt < 3; ++tt) SN[1][tt] = mfma(kf[6], qf[2][tt], SN[1][tt]);
#pragma unroll
      for (int tt = 0; tt < 3; ++tt) SN[1][tt] = mfma(kf[7], qf[3][tt], SN[1][tt]);
    }
    SB();
    if (__builtin_amdgcn_readfirstlane(flagv)) {
      float m[3];
      head_max(SC, m);
#pragma unroll
      for (int tt = 0; tt < 3; ++tt) {
        const float d = fmaxf(m[tt], 0.f);
        const float alpha = ex2(-d);
        const f32x4 d4 = {d, d, d, d};
        lsum[tt] *= alpha;
#pragma unroll
        for (int dt = 0; dt < 4; ++dt) O[dt][tt] *= alpha;
        nm[tt] -= d4;
        SC[0][tt] -= d4;
        SC[1][tt] -= d4;
        if constexpr (!LAST) { SN[0][tt] -= d4; SN[1][tt] -= d4; }
        Pown[tt] = p_put(tt, p_unit(SC[0][tt]), p_unit(SC[1][tt]));
        ps[tt] = mfma(ones, Pown[tt], zero4);
      }
      lds_barrier();
    }
    SB();
#pragma unroll
    for (int tt = 0; tt < 3; ++tt) Ppar[tt] = lds_ld128((pw ^ 1024u) + tt * 2048);
#pragma unroll
    for (int dt = 0; dt < 4; ++dt) vg[dt] = v_frag((va[dt] ^ 8192u) + vo);
#pragma unroll
    for (int tt = 0; tt < 3; ++tt) lsum[tt] += ps[tt][0];
    SB();
    pv3(0, vf[0], Pown);
    if constexpr (!LAST) LDMA(req_issue(IC(0), rq));
    SB();
    pv3(1, vf[1], Pown);
    if constexpr (!LAST) LDMA(req_issue(IC(1), rq));
    SB();
    pv3(2, vf[2], Pown);
    if constexpr (!LAST) LDMA(req_issue(IC(2), rq));
    SB();
    pv3(3, vf[3], Pown);
    if constexpr (!LAST) LDMA(req_issue(IC(3), rq));
    SB();
    pv3(0, vg[0], Ppar);
    pv3(1, vg[1], Ppar);
    if constexpr (LAST) {
      pv3(2, vg[2], Ppar);
      pv3(3, vg[3], Ppar);
    }
    SB();
  };
  auto wsrc = [&](int wi) { return wobase + (size_t)(wi >> 1) * 128 * 1024 + (wi & 1) * 256; };
  #define LAUNDER(v) asm volatile("" : "+v"(v))
  auto reqV = [&](int t) { LAUNDER(vxb); return Req{vbase + (size_t)t * 32768, 0u, 0x80u, 0u, 0x80u, vxb, false}; };
  auto reqK = [&](int t) { LAUNDER(vyb); return Req{kbase + (size_t)t * 32768, 0u, 0xC0u, 0x80u, 0x40u, vyb, false}; };
  auto reqW = [&](int wi) { LAUNDER(wvb); return Req{wsrc(wi), 3072u, 0x80u, 0u, 0x80u, wvb, true}; };

#pragma unroll 1
  for (int t2 = 0; t2 < 6; t2 += 2) {
    wait_barrier<8>();
    STAMP(3 + 2 * t2);
    if (t2 == 0) do_touch();
    xphase(IC(1), IC(3), std::false_type{}, SA, SB_, reqV(t2 + 1), t2);
    DSTAMP(25);
    wait_barrier<8>();
    STAMP(4 + 2 * t2);
    yphase(IC(2), IC(1), std::false_type{}, SA, SB_, reqK(t2 + 3), t2);
    DSTAMP(26);
    wait_barrier<8>();
    STAMP(5 + 2 * t2);
    xphase(IC(0), IC(2), std::false_type{}, SB_, SA, reqV(t2 + 2), t2 + 1);
    Req ry = t2 == 4 ? reqW(0) : reqK(t2 + 4);
    ry.shared = false;
    DSTAMP(27);
    wait_barrier<8>();
    STAMP(6 + 2 * t2);
    yphase(IC(3), IC(0), std::false_type{}, SB_, SA, ry, t2 + 1);
    DSTAMP(28);
  }
  wait_barrier<8>();
  STAMP(15);
  xphase(IC(1), IC(3), std::false_type{}, SA, SB_, reqV(7), 6);
  wait_barrier<8>();
  STAMP(16);
  yphase(IC(2), IC(1), std::false_type{}, SA, SB_, reqW(1), 6);
  wait_barrier<8>();
  STAMP(17);
  const unsigned epoch_v = __hip_atomic_load(epoch_ptr, __ATOMIC_RELAXED, __HIP_MEMORY_SCOPE_AGENT);
  SB();
  xphase(IC(0), IC(2), std::true_type{}, SB_, SA, reqW(2), 7);
  wait_barrier<8>();
  STAMP(18);
  yphase(IC(3), IC(3), std::true_type{}, SB_, SA, reqW(3), 7);

#pragma unroll
  for (int t = 0; t < 3; ++t)
    if (lane < 16) lds_stf(LS_OFF + ((hh * 4 + jq) * 3 + t) * 64 + lane * 4, lsum[t]);
  wait_barrier<0>();
  const unsigned tag = (unsigned)__builtin_amdgcn_readfirstlane(epoch_v) + 1u + (unsigned)pass;
  SB();
  STAMPR(19);
  set_m0(w4k + 3 * SLOT);
  static_for<0, 4>([&](auto ic) { req_issue(ic, reqW(3)); });
  const unsigned ow = PB + hh * 12288 + (2 * (jq & 1) + (jq >> 1)) * 3072 + lane * 16;
  const unsigned ox = PB + hh * 12288 + (2 * (jq & 1) + ((jq >> 1) ^ 1)) * 3072 + lane * 16;
  f32x4 Om[2][3];
  float lt[3];
  {
#pragma unroll
    for (int t = 0; t < 3; ++t) lds_st128(ow + t * 1024, __builtin_bit_cast(u32x4, O[2][t]));
#pragma unroll
    for (int t = 0; t < 3; ++t) {
      float s = 0.f;
#pragma unroll
      for (int j = 0; j < 4; ++j) s += lds_ldf(LS_OFF + ((hh * 4 + j) * 3 + t) * 64 + i16 * 4);
      lt[t] = s;
    }
    lds_barrier();
#pragma unroll
    for (int t = 0; t < 3; ++t) Om[0][t] = O[0][t] + __builtin_bit_cast(f32x4, lds_ld128(ox + t * 1024));
    SB();
#pragma unroll
    for (int t = 0; t < 3; ++t) lds_st128(ox + t * 1024, __builtin_bit_cast(u32x4, O[3][t]));
    lds_barrier();
#pragma unroll
    for (int t = 0; t < 3; ++t) Om[1][t] = O[1][t] + __builtin_bit_cast(f32x4, lds_ld128(ow + t * 1024));
  }

  STAMP(34);
  const __amdgpu_buffer_rsrc_t ws_rs = __builtin_amdgcn_make_buffer_rsrc((void*)ws, 0, WS_END, 0x00020000);
  const unsigned reg1 = WS_IN * (unsigned)(b + 1), reg2 = reg1 + WS_IN2;
  const unsigned lbid = (unsigned)bid & 31u;

  float lam[3];
  u32x4 mine[3];
#pragma unroll
  for (int t = 0; t < 3; ++t) {
    const float inv = __builtin_amdgcn_rcpf(lt[t]);
    lam[t] = __builtin_amdgcn_logf(lt[t]) - nm[t][0];
#pragma unroll
    for (int dt = 0; dt < 2; ++dt) Om[dt][t] *= inv;
    mine[t] = u32x4{pk_bf16(Om[0][t][0], Om[0][t][1]), pk_bf16(Om[0][t][2], Om[0][t][3]), pk_bf16(Om[1][t][0], Om[1][t][1]),
                    pk_bf16(Om[1][t][2], Om[1][t][3])};
  }
  {
    const float l0 = lam[0], l1 = lam[1], l2 = lam[2];
    const unsigned lw = __builtin_bit_cast(unsigned, (lane & 32) ? l2 : ((lane & 16) ? l1 : l0));
    unsigned h1, h2;
    hash13(mine[0], mine[1], mine[2], lw, tag, h1, h2);
    const unsigned dst = reg1 + (lbid ^ 16u) * 32768u + (unsigned)(w * 4096 + lane * 16);
    __builtin_amdgcn_raw_buffer_store_b128(mine[0], ws_rs, dst, 0, 16);
    __builtin_amdgcn_raw_buffer_store_b128(mine[1], ws_rs, dst + 1024, 0, 16);
    __builtin_amdgcn_raw_buffer_store_b128(mine[2], ws_rs, dst + 2048, 0, 16);
    __builtin_amdgcn_raw_buffer_store_b128(u32x4{lw, h1, h2, tag}, ws_rs, dst + 3072, 0, 16);
    SB();
    if (lane == 0) __builtin_amdgcn_raw_buffer_store_b32(tag, ws_rs, WS_HINT1 + (unsigned)((bid ^ 16) * 128 + w * 4), 0, 16);
  }
  STAMP(35);
  unsigned wa_[2];
  {
    const int q_ = (lane >> 2) & 3, p = lane & 3;
#pragma unroll
    for (int dt = 0; dt < 2; ++dt)
      wa_[dt] = hh * SLOT + (4 * g + q_) * 256 + (((4 * jq + 2 * dt + (p >> 1)) ^ (2 * ((4 * g + q_) & 7))) << 4) + 8 * (p & 1);
  }
  u32x4 wf[8][2];
#pragma unroll
  for (int st = 0; st < 4; ++st)
#pragma unroll
    for (int dt = 0; dt < 2; ++dt) wf[st][dt] = v_frag(wa_[dt] + st * 8192);
  SB();
  u32x4 th[4];
  STAMP(36);
  recv4<1024, 37, HOP1_AUX>(ws_rs, WS_HINT1 + (unsigned)(bid * 128 + w * 4), reg1 + lbid * 32768u + (unsigned)(w * 4096 + lane * 16), tag, th, lane, w);
  STAMPR(20);
  {
    const unsigned u = th[3][0];
    auto a = __builtin_amdgcn_permlane32_swap(u, u, false, false);
    auto c0 = __builtin_amdgcn_permlane16_swap(a[0], a[0], false, false);
    auto c1 = __builtin_amdgcn_permlane16_swap(a[1], a[1], false, false);
    const float lb[3] = {u2f(c0[0]), u2f(c0[1]), u2f(c1[0])};
#pragma unroll
    for (int t = 0; t < 3; ++t) {
      const float mx = fmaxf(lam[t], lb[t]);
      float wa = ex2(lam[t] - mx), wb = ex2(lb[t] - mx);
      const float inv = __builtin_amdgcn_rcpf(wa + wb);
      wa *= inv;
      wb *= inv;
      float m[8];
#pragma unroll
      for (int k = 0; k < 4; ++k) {
        m[2 * k] = wa * Om[k >> 1][t][2 * (k & 1)] + wb * bf_lo(th[t][k]);
        m[2 * k + 1] = wa * Om[k >> 1][t][2 * (k & 1) + 1] + wb * bf_hi(th[t][k]);
      }
      lds_st128(ow + t * 1024, u32x4{pk_bf16(m[0], m[1]), pk_bf16(m[2], m[3]), pk_bf16(m[4], m[5]), pk_bf16(m[6], m[7])});
    }
  }
  wait_barrier<0>();
  STAMP(21);

  const unsigned pr = PB + lane * 16;
  f32x4 A[2][3];
#pragma unroll
  for (int dt = 0; dt < 2; ++dt)
#pragma unroll
    for (int t = 0; t < 3; ++t) A[dt][t] = zero4;
  {
    u32x4 pf[8][3];
#pragma unroll
    for (int st = 0; st < 4; ++st)
#pragma unroll
      for (int t = 0; t < 3; ++t) pf[st][t] = lds_ld128(pr + st * 3072 + t * 1024);
#pragma unroll
    for (int st = 4; st < 8; ++st) {
#pragma unroll
      for (int dt = 0; dt < 2; ++dt) wf[st][dt] = v_frag(wa_[dt] + 2 * SLOT + (st - 4) * 8192);
#pragma unroll
      for (int t = 0; t < 3; ++t) pf[st][t] = lds_ld128(pr + st * 3072 + t * 1024);
    }
    SB();
#pragma unroll
    for (int st = 0; st < 8; ++st)
#pragma unroll
      for (int dt = 0; dt < 2; ++dt)
#pragma unroll
        for (int t = 0; t < 3; ++t) A[dt][t] = mfma(wf[st][dt], pf[st][t], A[dt][t]);
  }
  STAMP(22);

  {
    u32x4 f[3];
#pragma unroll
    for (int t = 0; t < 3; ++t)
      f[t] = u32x4{pk_bf16(A[0][t][0], A[0][t][1]), pk_bf16(A[0][t][2], A[0][t][3]), pk_bf16(A[1][t][0], A[1][t][1]),
                   pk_bf16(A[1][t][2], A[1][t][3])};
    unsigned h1, h2;
    hash13(f[0], f[1], f[2], 0u, tag, h1, h2);
    const int rho = 2 * w + (i16 >> 3);
    const int rbid = (rho >> 1) + 8 * ((rho & 1) + 2 * kh + 4 * b);
    const int pidx = kvh * 2 + hp;
    const unsigned dst = reg2 + (unsigned)((rbid & 31) * 32768 + pidx * 2048 + (8 * g + (i16 & 7)) * 16);
    __builtin_amdgcn_raw_buffer_store_b128(f[0], ws_rs, dst, 0, 16);
    __builtin_amdgcn_raw_buffer_store_b128(f[1], ws_rs, dst + 512, 0, 16);
    __builtin_amdgcn_raw_buffer_store_b128(f[2], ws_rs, dst + 1024, 0, 16);
    __builtin_amdgcn_raw_buffer_store_b128(u32x4{0u, h1, h2, tag}, ws_rs, dst + 1536, 0, 16);
    SB();
    if ((lane & 55) == 0) __builtin_amdgcn_raw_buffer_store_b32(tag, ws_rs, WS_HINT2 + (unsigned)(rbid * 128 + pidx * 4), 0, 16);
  }
  {
    STAMP(39);
    recv4<512, 40, HOP2_AUX>(ws_rs, WS_HINT2 + (unsigned)(bid * 128 + (tid >> 5) * 4), reg2 + lbid * 32768u + (unsigned)((tid >> 5) * 2048 + (tid & 31) * 16), tag, th, lane, w);
    STAMPR(23);
#pragma unroll
    for (int t = 0; t < 3; ++t) {
      f32x4 r;
#pragma unroll
      for (int k = 0; k < 2; ++k) {
        const float a0 = half_sum(bf_lo(th[t][k])), a1 = half_sum(bf_hi(th[t][k]));
        const float b0 = half_sum(bf_lo(th[t][2 + k])), b1 = half_sum(bf_hi(th[t][2 + k]));
        r[2 * k] = lane < 32 ? a0 : b0;
        r[2 * k + 1] = lane < 32 ? a1 : b1;
      }
      lds_st128(w * 3072 + t * 1024 + lane * 16, __builtin_bit_cast(u32x4, r));
    }
  }
  lds_barrier();
  if (tid < 192) {
    const int t = tid >> 6;
    f32x4 s = __builtin_bit_cast(f32x4, lds_ld128(t * 1024 + lane * 16));
#pragma unroll
    for (int v = 1; v < 8; ++v) s += __builtin_bit_cast(f32x4, lds_ld128(v * 3072 + t * 1024 + lane * 16));
    const int rho = xk * 2 + hp;
    const int nt = lane >> 5, l32 = lane & 31, gg = l32 >> 3, q = 8 * (rho & 1) + (l32 & 7);
    const int col = 256 * kh + 32 * (rho >> 1) + 16 * nt + 4 * gg;
    u32x2 o = {pk_bf16(s[0], s[1]), pk_bf16(s[2], s[3])};
    *(u32x2*)(out + ((size_t)b * SQ + 16 * t + q) * DM + col) = o;
  }
  STAMPR(24);
  STAMP_DUMP();
  if (pass == NPASS - 1 && tid == 0 && xk == 0 && hp == 0 && kh == 0) __hip_atomic_store(epoch_ptr, tag, __ATOMIC_RELAXED, __HIP_MEMORY_SCOPE_AGENT);
  asm volatile("s_waitcnt vmcnt(0) lgkmcnt(0)" ::: "memory");
  }
}

}

extern "C" void kernel_launch(void* const* d_in, const int* in_sizes, int n_in, void* d_out, int out_size, void* d_ws,
                              size_t ws_size, hipStream_t stream) {
  (void)in_sizes; (void)n_in; (void)out_size; (void)ws_size;
  (void)hipFuncSetAttribute((const void*)gqa_fused_kernel, hipFuncAttributeMaxDynamicSharedMemorySize, LDS_BYTES);
  gqa_fused_kernel<<<256, 512, LDS_BYTES, stream>>>((const uint16_t*)d_in[0], (const uint16_t*)d_in[1], (const uint16_t*)d_in[2],
                                                    (const uint16_t*)d_in[3], (const uint16_t*)d_in[4], (uint16_t*)d_out,
                                                    (unsigned char*)d_ws);
}
```

```cpp
#include <hip/hip_runtime.h>
#include <hip/hip_bf16.h>
#include <stdint.h>
#include <type_traits>

#define AS3 __attribute__((address_space(3)))
typedef __attribute__((ext_vector_type(8))) __bf16 bf16x8;
typedef __attribute__((ext_vector_type(2))) __bf16 bf16x2;
typedef __attribute__((ext_vector_type(4))) float f32x4;
typedef __attribute__((ext_vector_type(2))) float f32x2;
typedef __attribute__((ext_vector_type(4))) unsigned u32x4;
typedef __attribute__((ext_vector_type(2))) unsigned u32x2;
typedef __attribute__((ext_vector_type(4))) short i16x4;

#define STAMP(i) do {} while (0)
#define STAMPR(i) do {} while (0)
#define STAMP_DUMP() do { __builtin_amdgcn_s_waitcnt(0xc07f);} while (0)
#define SB() __builtin_amdgcn_sched_barrier(0)
#define IC(v) std::integral_constant<int, (v)>{}
#define HOP1_AUX 0
#define HOP2_AUX 0
#define MAPK 3
#define TOUCH_AT 1
#define RQ4 0
#define PF_CODE 1
#define PF_KV 0
#define PF_OUT 1
#define HSPREAD 1
#define OR1 1
#define OUTPOL 0
#define PF_WS 0
#define NTOUCH (PF_CODE + PF_KV + (PF_WS == 1))
#define KVPOL " nt"
#define LDMA(x) x
#define DSTAMP(i) do {} while (0)

namespace {

constexpr int SQ = 48, DM = 512, SK = 2048, DH = 128, NQ = 4096;

constexpr unsigned SLOT = 32768;
constexpr unsigned PB = 131072;
constexpr unsigned MISC = 155648;
constexpr unsigned MX_OFF = MISC;
constexpr unsigned LS_OFF = MISC + 1536;
constexpr unsigned FL_OFF = MISC + 3072;
constexpr unsigned TOUCH_OFF = MISC + 3328;
constexpr unsigned XIMG = 98304;
constexpr unsigned LDS_BYTES = 163840;

constexpr unsigned WS_EPOCH = 0;
constexpr unsigned WS_HINT1 = 4096;
constexpr unsigned WS_HINT2 = WS_HINT1 + 262144;
constexpr unsigned H1W = 1024, H1S = 128, H2W = 2048, H2S = 128;
constexpr unsigned WS_IN = 2u << 20;
constexpr unsigned WS_IN2 = 1u << 20;
constexpr unsigned WS_END = WS_IN * 9;

constexpr float SCL = 0.08838834764831845f * 1.4426950408889634f;
constexpr unsigned SUM_CAP = 0x47800000u;
constexpr unsigned SPIN_CAP = 1u << 16;

__device__ __forceinline__ u32x4 lds_ld128(unsigned a) { return *(const AS3 u32x4*)(size_t)a; }
__device__ __forceinline__ void lds_st128(unsigned a, u32x4 v) { *(AS3 u32x4*)(size_t)a = v; }
__device__ __forceinline__ float lds_ldf(unsigned a) { return *(const AS3 float*)(size_t)a; }
__device__ __forceinline__ void lds_stf(unsigned a, float v) { *(AS3 float*)(size_t)a = v; }
__device__ __forceinline__ unsigned lds_ldu(unsigned a) { return *(const AS3 unsigned*)(size_t)a; }
__device__ __forceinline__ void lds_stu(unsigned a, unsigned v) { *(AS3 unsigned*)(size_t)a = v; }
__device__ __forceinline__ u32x2 lds_tr(unsigned a) {
  i16x4 r = __builtin_amdgcn_ds_read_tr16_b64_v4i16((AS3 i16x4*)(size_t)a);
  return __builtin_bit_cast(u32x2, r);
}
__device__ __forceinline__ f32x4 mfma(u32x4 a, u32x4 b, f32x4 c) {
  return __builtin_amdgcn_mfma_f32_16x16x32_bf16(__builtin_bit_cast(bf16x8, a), __builtin_bit_cast(bf16x8, b), c, 0, 0, 0);
}
__device__ __forceinline__ unsigned pk_bf16(float lo, float hi) {
  f32x2 v = {lo, hi};
  bf16x2 r = __builtin_convertvector(v, bf16x2);
  return __builtin_bit_cast(unsigned, r);
}
__device__ __forceinline__ float u2f(unsigned u) { return __builtin_bit_cast(float, u); }
__device__ __forceinline__ unsigned f2u(float f) { return __builtin_bit_cast(unsigned, f); }
__device__ __forceinline__ float bf_lo(unsigned u) { return __builtin_bit_cast(float, u << 16); }
__device__ __forceinline__ float bf_hi(unsigned u) { return __builtin_bit_cast(float, u & 0xffff0000u); }
__device__ __forceinline__ float ex2(float v) { return __builtin_amdgcn_exp2f(v); }

template <int OFF>
__device__ __forceinline__ void dma_nt(unsigned voff, const void* sbase, unsigned m0v) {
  asm volatile("s_mov_b32 m0, %2\n\ts_nop 0\n\tglobal_load_lds_dwordx4 %0, %1 offset:%3" KVPOL
               :: "v"(voff), "s"(sbase), "s"(m0v), "n"(OFF) : "memory");
}
template <int OFF>
__device__ __forceinline__ void dma_def(unsigned voff, const void* sbase, unsigned m0v) {
  asm volatile("s_mov_b32 m0, %2\n\ts_nop 0\n\tglobal_load_lds_dwordx4 %0, %1 offset:%3"
               :: "v"(voff), "s"(sbase), "s"(m0v), "n"(OFF) : "memory");
}
__device__ __forceinline__ void set_m0(unsigned m0v) { asm volatile("s_mov_b32 m0, %0\n\ts_nop 0" ::"s"(m0v) : "memory"); }
template <int OFF>
__device__ __forceinline__ void dma_0(unsigned vb, const void* sbase, bool shared) {
  if (shared) asm volatile("global_load_lds_dwordx4 %0, %1 offset:%2" ::"v"(vb), "s"(sbase), "n"(OFF) : "memory");
  else asm volatile("global_load_lds_dwordx4 %0, %1 offset:%2" KVPOL ::"v"(vb), "s"(sbase), "n"(OFF) : "memory");
}
__device__ __forceinline__ void touch(unsigned voff, const void* sbase, unsigned m0v) {
  asm volatile("s_mov_b32 m0, %2\n\ts_nop 0\n\tglobal_load_lds_dword %0, %1 sc1" ::"v"(voff), "s"(sbase), "s"(m0v) : "memory");
}
__device__ __forceinline__ void touch_s(unsigned voff, const void* sbase, unsigned m0v) {
  asm volatile("s_mov_b32 m0, %2\n\ts_nop 0\n\tglobal_load_lds_dword %0, %1" ::"v"(voff), "s"(sbase), "s"(m0v) : "memory");
}
__device__ __forceinline__ void touch_v(unsigned long long addr, unsigned m0v) {
  asm volatile("s_mov_b32 m0, %1\n\ts_nop 0\n\tglobal_load_lds_dword %0, off" ::"v"(addr), "s"(m0v) : "memory");
}
template <int I, int N, class F>
__device__ __forceinline__ void static_for(F&& f) {
  if constexpr (I < N) {
    f(std::integral_constant<int, I>{});
    static_for<I + 1, N>(f);
  }
}
template <int N>
__device__ __forceinline__ void wait_barrier() {
  asm volatile("s_waitcnt vmcnt(%0) lgkmcnt(0)\n\ts_barrier" ::"n"(N) : "memory");
}
__device__ __forceinline__ void lds_barrier() { asm volatile("s_waitcnt lgkmcnt(0)\n\ts_barrier" ::: "memory"); }

__device__ __forceinline__ float grp_max(float v) {
  unsigned u = __builtin_bit_cast(unsigned, v);
  auto a = __builtin_amdgcn_permlane16_swap(u, u, false, false);
  v = fmaxf(u2f(a[0]), u2f(a[1]));
  u = __builtin_bit_cast(unsigned, v);
  auto c = __builtin_amdgcn_permlane32_swap(u, u, false, false);
  return fmaxf(u2f(c[0]), u2f(c[1]));
}
__device__ __forceinline__ float half_sum(float v) {
  unsigned u = __builtin_bit_cast(unsigned, v);
  auto c = __builtin_amdgcn_permlane32_swap(u, u, false, false);
  return u2f(c[0]) + u2f(c[1]);
}

template <int R>
__device__ __forceinline__ unsigned rot(unsigned h) { return __builtin_amdgcn_alignbit(h, h, R); }
__device__ __forceinline__ void hash13(const u32x4& a, const u32x4& b, const u32x4& c, unsigned d, unsigned tag,
                                       unsigned& h1, unsigned& h2) {
  auto x3 = [](unsigned p, unsigned q, unsigned r) { return __builtin_amdgcn_bitop3_b32(p, q, r, 0x96); };
  const unsigned x0 = x3(a[0], rot<5>(a[1]), rot<10>(a[2])), x1 = x3(rot<15>(a[3]), rot<20>(b[0]), rot<25>(b[1]));
  const unsigned x2 = x3(rot<30>(b[2]), rot<3>(b[3]), rot<8>(c[0])), x4 = x3(rot<13>(c[1]), rot<18>(c[2]), rot<23>(c[3]));
  h1 = x3(x3(x0, x1, x2), x4, rot<28>(d) ^ (tag * 0x9E3779B1u + 0x7F4A7C15u));
  h2 = (a[0] + a[1] + a[2]) + (a[3] + b[0] + b[1]) + (b[2] + b[3] + c[0]) + (c[1] + c[2] + c[3]) + (d + 2u * tag + 1u);
}

template <int STR, int SI, int MODE>
__device__ __forceinline__ void recv4(const __amdgpu_buffer_rsrc_t& rs, unsigned hint, unsigned src, unsigned tag, u32x4 (&th)[4], int lane, int w) {
  unsigned n1 = 0, n2 = 0;
  auto attempt = [&]() {
    asm volatile("" ::: "memory");
#pragma unroll
    for (int k = 0; k < 4; ++k) th[k] = __builtin_amdgcn_raw_buffer_load_b128(rs, src + k * STR, 0, 16);
    unsigned h1, h2;
    hash13(th[0], th[1], th[2], th[3][0], tag, h1, h2);
    const bool ok = (h1 == th[3][1]) & (h2 == th[3][2]);
    ++n2;
    return __builtin_amdgcn_ballot_w64(ok) == ~0ull;
  };
  bool done = false;
  if constexpr (MODE & 1) done = attempt();
  if (!done) {
    if constexpr (!(MODE & 2)) {
#pragma unroll 1
      for (unsigned s2 = 0; s2 < SPIN_CAP; ++s2) {
        asm volatile("" ::: "memory");
        const unsigned tg = __builtin_amdgcn_raw_buffer_load_b32(rs, hint, 0, 16);
        ++n1;
        if (__builtin_amdgcn_ballot_w64(tg == tag) == ~0ull) break;
        __builtin_amdgcn_s_sleep(1);
      }
    }
    STAMP(SI);
#pragma unroll 1
    for (unsigned spin = 0; spin < SPIN_CAP; ++spin) {
      if (attempt()) break;
    }
  } else {
    STAMP(SI);
  }
  (void)n1; (void)n2; (void)lane; (void)w;
}

__global__ __launch_bounds__(512, 2) void gqa_fused_kernel(const uint16_t* __restrict__ x, const uint16_t* __restrict__ kc,
                                                           const uint16_t* __restrict__ vc, const uint16_t* __restrict__ wq,
                                                           const uint16_t* __restrict__ wo, uint16_t* __restrict__ out,
                                                           unsigned char* __restrict__ ws) {
  const int tid = threadIdx.x;
  const int lane = tid & 63;
  const int w = __builtin_amdgcn_readfirstlane(tid >> 6);
  const int bid = blockIdx.x;
  const int xk = bid & 7, slot = bid >> 3, hp = slot & 1, kh = (slot >> 1) & 1, b = slot >> 2;
  const int kvh = (xk & 4) | ((xk + b) & 3);
  const int g = lane >> 4, i16 = lane & 15;
  const int hh = w >> 2, jq = w & 3;
  const int head0 = kvh * 4 + hp * 2;

  STAMPR(0);
  unsigned* epoch_ptr = (unsigned*)(ws + WS_EPOCH + (size_t)b * 256);

  const char* kbase = (const char*)kc + ((size_t)(b * 8 + kvh) * SK + kh * 1024) * (DH * 2);
  const char* vbase = (const char*)vc + ((size_t)(b * 8 + kvh) * SK + kh * 1024) * (DH * 2);
  const char* wobase = (const char*)wo + ((size_t)head0 * DH * DM + kh * 256) * 2;

  unsigned voffK[4], voffV[2];
#pragma unroll
  for (int i = 0; i < 4; ++i) voffK[i] = (unsigned)(w * 4096 + g * 256 + ((i16 ^ (4 * i + g) ^ ((i & 1) << 3)) << 4));
#pragma unroll
  for (int i = 0; i < 2; ++i) voffV[i] = (unsigned)(w * 4096 + g * 256 + ((i16 ^ (2 * (4 * i + g))) << 4));
  const unsigned w4k = (unsigned)(w * 4096);
  u32x4 qf[4][3], kf[8];
  f32x4 O[4][3];
  float lsum[3];
  u32x4 Ppar[3], vg[4];
  unsigned ka[4];
#pragma unroll
  for (int s = 0; s < 4; ++s) ka[s] = (32 * jq + i16) * 256 + (((4 * g + s) ^ i16 ^ ((i16 & 4) << 1)) << 4);

  auto do_touch = [&]() {
    if (w == 0) touch(lane >= 32 ? WS_IN * (unsigned)(b + 1) + (unsigned)(lane - 32) * 65536u
                                 : (lane >= 24 ? WS_EPOCH + (unsigned)b * 256u
                                               : (lane >= 8 ? WS_HINT2 + (unsigned)bid * H2W + (unsigned)(lane - 8) * H2S
                                                            : WS_HINT1 + (unsigned)bid * H1W + (unsigned)lane * H1S)), ws, TOUCH_OFF);
  };
  auto ws_touch = [&]() {
    touch((unsigned)lane * 128u + (unsigned)(w & 3) * 8192u + (w >= 4 ? WS_IN2 : 0u) + WS_IN * (unsigned)(b + 1) + ((unsigned)bid & 31u) * 32768u,
          ws, TOUCH_OFF);
  };
  {
    STAMP(42);
    const char* xb = (const char*)x + (size_t)b * SQ * DM * 2;
    const int rot = (slot >> 1) & 7;
    const char* wqb = (const char*)wq + head0 * (DH * 2);
    const unsigned voffQ = (unsigned)((8 * w + (lane >> 5)) * (NQ * 2) + (((lane & 31) ^ (2 * ((lane >> 5) + 4 * (w & 1)))) << 4));
    const int xrow = 6 * w + (lane >> 3);
    const unsigned voffX = (unsigned)(xrow * 1024 + (((lane & 7) ^ ((xrow >> 1) & 7)) << 4));
    auto rq_piece = [&](auto sc, auto pc) {
      constexpr int st = decltype(sc)::value, i = decltype(pc)::value - 1;
      const int tt = (st + rot) & 7;
      if constexpr (i < 0) {
        if (lane < 48) dma_def<0>(voffX, xb + tt * 128, (unsigned)(PB + (st & 3) * 6144 + w * 768));
      } else {
        const char* src = wqb + (size_t)(tt * 64) * (NQ * 2);
        dma_def<i * 1024>(voffQ ^ ((i & 1) * 64u), src + i * (2 * NQ * 2 - 1024), w4k + (st & 3) * SLOT);
      }
    };
    auto rq_all = [&](auto sc) { static_for<0, 5>([&](auto pc) { rq_piece(sc, pc); }); };
    auto prefetch = [&]() {
      {
        const unsigned long long pc = __builtin_amdgcn_s_getpc() & ~127ull;
        unsigned long long a = (unsigned long long)(size_t)kbase;
        if (w == 0) {
          if (lane < 38) a = pc + (unsigned)(((slot & 3) * 38 + lane) * 128);
          else if (lane >= 40) {
            const int l = lane - 40, rho = xk * 2 + hp;
            a = (unsigned long long)(size_t)(out + ((size_t)b * SQ + 16 * (l >> 3) + 8 * (rho & 1) + (l & 7)) * DM + 256 * kh + 32 * (rho >> 1));
          }
        } else if (w == 1) {
          if (lane < 4) a = (unsigned long long)(size_t)vbase;
          else if (lane < 8) a = (unsigned long long)(size_t)wobase;
        }
        touch_v(a, TOUCH_OFF);
      }
      static_for<0, PF_KV>([&](auto jc) {
        constexpr int j = decltype(jc)::value;
        const char* src = j == 0 ? kbase + w * 8192 : (w < 4 ? vbase + w * 8192 : kbase + 65536 + (w - 4) * 8192);
        touch_s((unsigned)lane * 128u, src, TOUCH_OFF);
      });
    };
    rq_all(IC(0));
    rq_all(IC(1));
    SB();
    STAMP(43);
    SB();
    if (tid < 16) lds_stu(FL_OFF + tid * 4, 0u);
#pragma unroll
    for (int t = 0; t < 3; ++t) {
#pragma unroll
      for (int dt = 0; dt < 4; ++dt) {
        O[dt][t] = f32x4{0.f, 0.f, 0.f, 0.f};
        asm volatile("" : "+v"(O[dt][t]));
      }
      lsum[t] = 0.f;
      Ppar[t] = u32x4{0u, 0u, 0u, 0u};
      asm volatile("" : "+v"(lsum[t]), "+v"(Ppar[t]));
    }
    vg[2] = u32x4{0u, 0u, 0u, 0u};
    vg[3] = u32x4{0u, 0u, 0u, 0u};
    asm volatile("" : "+v"(vg[2]), "+v"(vg[3]));
    unsigned aq[2];
    {
      const int q_ = (lane >> 2) & 3, p = lane & 3;
#pragma unroll
      for (int dt = 0; dt < 2; ++dt)
        aq[dt] = (8 * g + q_) * 512 + (((4 * w + 2 * dt + (p >> 1)) ^ (2 * (q_ + 4 * (g & 1)))) << 4) + 8 * (p & 1);
    }
    const unsigned xl = PB + i16 * 128 + ((g ^ (i16 >> 1)) << 4);
    f32x4 acc[2][3];
#pragma unroll
    for (int dt = 0; dt < 2; ++dt)
#pragma unroll
      for (int t = 0; t < 3; ++t) acc[dt][t] = f32x4{0.f, 0.f, 0.f, 0.f};
    u32x4 af[2][2][2], bf[2][2][3];
    auto frag_a = [&](auto sc, int e) {
      constexpr int st = decltype(sc)::value, pp = st & 1;
      constexpr unsigned so = (st & 3) * SLOT;
#pragma unroll
      for (int dt = 0; dt < 2; ++dt) {
        const u32x2 v0 = lds_tr(aq[dt] + so + e * 16384), v1 = lds_tr(aq[dt] + so + e * 16384 + 2048);
        af[pp][e][dt] = u32x4{v0[0], v0[1], v1[0], v1[1]};
      }
    };
    auto frag_b = [&](auto sc, int e) {
      constexpr int st = decltype(sc)::value, pp = st & 1;
      constexpr unsigned xo = (st & 3) * 6144;
#pragma unroll
      for (int t = 0; t < 3; ++t) bf[pp][e][t] = lds_ld128((xl ^ (e * 64u)) + xo + t * 2048);
    };
    auto frags = [&](auto sc) { frag_a(sc, 0); frag_b(sc, 0); frag_a(sc, 1); frag_b(sc, 1); };
    auto qm = [&](int pp, int e, int dt) {
#pragma unroll
      for (int t = 0; t < 3; ++t) acc[dt][t] = mfma(af[pp][e][dt], bf[pp][e][t], acc[dt][t]);
    };
    wait_barrier<5>();
    STAMP(29);
    rq_all(IC(2));
    SB();
    frags(IC(0));
    SB();
    rq_all(IC(3));
    prefetch();
    SB();
    static_for<0, 7>([&](auto sc) {
      constexpr int st = decltype(sc)::value, pp = st & 1;
      if constexpr (st <= 2) wait_barrier<10 + NTOUCH>(); else if constexpr (st <= 4) wait_barrier<10>(); else if constexpr (st == 5) wait_barrier<9>(); else wait_barrier<8>();
      SB();
      STAMP(44 + st);
      auto piece = [&](auto pc) {
        constexpr int p = decltype(pc)::value;
        if constexpr (st <= 3) rq_piece(IC(st + 4), pc);
        else if constexpr (p >= 1) {
          if constexpr (st == 4) dma_nt<(p - 1) * 1024>(voffK[p - 1], kbase, w4k);
          if constexpr (st == 5) dma_nt<(p - 1) * 1024>(voffK[p - 1], kbase + 32768, w4k + SLOT);
          if constexpr (st == 6) dma_nt<(p - 1) * 1024>(voffV[(p - 1) & 1], vbase, w4k + 2 * SLOT);
        }
        SB();
      };
      piece(IC(0));
      piece(IC(1));
      frag_a(IC(st + 1), 0); qm(pp, 0, 0); SB();
      piece(IC(2));
      frag_b(IC(st + 1), 0); qm(pp, 0, 1); SB();
      piece(IC(3));
      frag_a(IC(st + 1), 1); qm(pp, 1, 0); SB();
      piece(IC(4));
      frag_b(IC(st + 1), 1); qm(pp, 1, 1); SB();
    });
    STAMP(30);
    wait_barrier<8>();
#pragma unroll
    for (int c = 0; c < 8; ++c) kf[c] = lds_ld128(ka[c & 3] + (c >> 2) * 4096);
    SB();
    qm(1, 0, 0); qm(1, 0, 1); qm(1, 1, 0); qm(1, 1, 1);
#pragma unroll
    for (int dt = 0; dt < 2; ++dt)
#pragma unroll
      for (int t = 0; t < 3; ++t) {
        const u32x2 v = {pk_bf16(acc[dt][t][0] * SCL, acc[dt][t][1] * SCL), pk_bf16(acc[dt][t][2] * SCL, acc[dt][t][3] * SCL)};
        *(AS3 u32x2*)(size_t)(XIMG + hh * 12288 + t * 4096 + (4 * jq + 2 * dt + (g >> 1)) * 256 + i16 * 16 + (g & 1) * 8) = v;
      }
    STAMP(31);
    lds_barrier();
#pragma unroll
    for (int s = 0; s < 4; ++s)
#pragma unroll
      for (int t = 0; t < 3; ++t) qf[s][t] = lds_ld128(XIMG + hh * 12288 + t * 4096 + (4 * g + s) * 256 + i16 * 16);
  }
  STAMPR(1);

  unsigned va[4];
  {
    const int q_ = (lane >> 2) & 3, p = lane & 3;
#pragma unroll
    for (int dt = 0; dt < 4; ++dt)
      va[dt] = 2 * SLOT + (32 * jq + 4 * g + q_) * 256 + (((8 * (jq & 1) + 2 * (dt ^ (jq & 2)) + (p >> 1)) ^ (2 * ((4 * g + q_) & 7))) << 4) + 8 * (p & 1);
  }
  const unsigned pw = PB + hh * 12288 + (jq >> 1) * 6144 + (jq & 1) * 1024 + lane * 16;

  f32x4 nm[3];
  f32x4 ps[3];
  const u32x4 ones = {0x3F803F80u, 0x3F803F80u, 0x3F803F80u, 0x3F803F80u};
  const f32x4 zero4 = {0.f, 0.f, 0.f, 0.f};

  auto tile_max = [&](const f32x4(&S)[2][3], int t) {
    float m = fmaxf(fmaxf(S[0][t][0], S[0][t][1]), S[0][t][2]);
    m = fmaxf(fmaxf(m, S[0][t][3]), S[1][t][0]);
    m = fmaxf(fmaxf(m, S[1][t][1]), S[1][t][2]);
    return fmaxf(m, S[1][t][3]);
  };
  auto head_max = [&](const f32x4(&S)[2][3], float (&m)[3]) {
#pragma unroll
    for (int t = 0; t < 3; ++t) {
      const float v = grp_max(tile_max(S, t));
      if (lane < 16) lds_stf(MX_OFF + ((hh * 4 + jq) * 3 + t) * 64 + lane * 4, v);
    }
    lds_barrier();
#pragma unroll
    for (int t = 0; t < 3; ++t) {
      float v = lds_ldf(MX_OFF + ((hh * 4 + 0) * 3 + t) * 64 + i16 * 4);
#pragma unroll
      for (int j = 1; j < 4; ++j) v = fmaxf(v, lds_ldf(MX_OFF + ((hh * 4 + j) * 3 + t) * 64 + i16 * 4));
      m[t] = v;
    }
  };
  auto p_unit = [&](const f32x4& s) { return u32x2{pk_bf16(ex2(s[0]), ex2(s[1])), pk_bf16(ex2(s[2]), ex2(s[3]))}; };
  auto p_put = [&](int t, u32x2 a, u32x2 c) {
    const u32x4 f = {a[0], a[1], c[0], c[1]};
    lds_st128(pw + t * 2048, f);
    return f;
  };
  auto v_frag = [&](unsigned a) {
    const u32x2 v0 = lds_tr(a), v1 = lds_tr(a + 4096);
    return u32x4{v0[0], v0[1], v1[0], v1[1]};
  };

  unsigned vxb = voffV[0], vyb = voffK[0];
  unsigned wvb = (unsigned)((16 * w + g) * 1024 + ((i16 ^ (2 * g)) << 4));

  f32x4 SA[2][3], SB_[2][3];
  u32x4 Pown[3];
  STAMP(2);
#pragma unroll
  for (int c = 0; c < 8; ++c)
#pragma unroll
    for (int t = 0; t < 3; ++t) SA[c >> 2][t] = mfma(kf[c], qf[c & 3][t], (c & 3) == 0 ? zero4 : SA[c >> 2][t]);
  {
    float m[3];
    head_max(SA, m);
    static_for<0, 4>([&](auto ic) { constexpr int i = decltype(ic)::value; dma_nt<i * 1024>(voffK[i], kbase + 2 * 32768, w4k); });
#pragma unroll
    for (int t = 0; t < 3; ++t) {
      nm[t] = f32x4{-m[t], -m[t], -m[t], -m[t]};
      SA[0][t] += nm[t];
      SA[1][t] += nm[t];
    }
  }

  struct Req { const char* src; unsigned ex, x1, x2, x3, vb; bool shared; };
  auto req_issue = [&](auto ic, const Req& r) {
    constexpr int i = decltype(ic)::value;
    if constexpr (i == 0) dma_0<0>(r.vb, r.src, r.shared);
    else dma_0<i * 1024>(r.vb ^ (i == 1 ? r.x1 : (i == 2 ? r.x2 : r.x3)), r.src + i * r.ex, r.shared);
  };
  auto pv3 = [&](int dt, const u32x4& v, const u32x4 (&p)[3]) {
#pragma unroll
    for (int t = 0; t < 3; ++t) O[dt][t] = mfma(v, p[t], O[dt][t]);
  };
  auto xphase = [&](auto ksc, auto dsc, auto lastc, f32x4(&SC)[2][3], f32x4(&SN)[2][3], const Req& rq, int t) {
    constexpr unsigned ko = decltype(ksc)::value * SLOT;
    constexpr bool LAST = decltype(lastc)::value;
    LDMA(set_m0(w4k + decltype(dsc)::value * SLOT));
    auto qk = [&](auto cc) {
      constexpr int c = decltype(cc)::value, kt = c >> 2, s = c & 3;
      if constexpr (!LAST) {
#pragma unroll
        for (int tt = 0; tt < 3; ++tt) SN[kt][tt] = mfma(kf[c], qf[s][tt], s == 0 ? nm[tt] : SN[kt][tt]);
      }
    };
    if constexpr (!LAST) {
#pragma unroll
      for (int c = 0; c < 4; ++c) kf[c] = lds_ld128(ka[c] + ko);
    }
    SB();
    pv3(2, vg[2], Ppar);
    u32x2 a0 = p_unit(SC[0][0]);
    SB();
    pv3(3, vg[3], Ppar);
    u32x2 a1 = p_unit(SC[1][0]);
    Pown[0] = p_put(0, a0, a1);
    SB();
    if constexpr (!LAST) {
#pragma unroll
      for (int c = 4; c < 8; ++c) kf[c] = lds_ld128(ka[c & 3] + ko + 4096);
    }
    LDMA(req_issue(IC(0), rq));
    SB();
    qk(IC(0));
    a0 = p_unit(SC[0][1]);
    SB();
    qk(IC(1));
    a1 = p_unit(SC[1][1]);
    Pown[1] = p_put(1, a0, a1);
    ps[0] = mfma(ones, Pown[0], zero4);
    SB();
    LDMA(req_issue(IC(1), rq));
    SB();
    qk(IC(2));
    a0 = p_unit(SC[0][2]);
    SB();
    qk(IC(3));
    a1 = p_unit(SC[1][2]);
    Pown[2] = p_put(2, a0, a1);
    ps[1] = mfma(ones, Pown[1], zero4);
    SB();
    LDMA(req_issue(IC(2), rq));
    SB();
    qk(IC(4));
    ps[2] = mfma(ones, Pown[2], zero4);
    SB();
    LDMA(req_issue(IC(3), rq));
    SB();
    qk(IC(5));
    {
      const unsigned u0 = f2u(ps[0][0]), u1 = f2u(ps[1][0]), u2 = f2u(ps[2][0]);
      const unsigned um = max(max(u0, u1), u2);
      if (um > SUM_CAP) lds_stu(FL_OFF + t * 4, 1u);
    }
    SB();
  };
  auto yphase = [&](auto vsc, auto dsc, auto lastc, f32x4(&SC)[2][3], f32x4(&SN)[2][3], const Req& rq, int t) {
    constexpr unsigned vo = (decltype(vsc)::value - 2) * SLOT;
    constexpr bool LAST = decltype(lastc)::value;
    if constexpr (!LAST) LDMA(set_m0(w4k + decltype(dsc)::value * SLOT));
    const unsigned flagv = lds_ldu(FL_OFF + t * 4);
    SB();
    u32x4 vf[4];
#pragma unroll
    for (int dt = 0; dt < 4; ++dt) vf[dt] = v_frag(va[dt] + vo);
    SB();
    if constexpr (!LAST) {
#pragma unroll
      for (int tt = 0; tt < 3; ++tt) SN[1][tt] = mfma(kf[6], qf[2][tt], SN[1][tt]);
#pragma unroll
      for (int tt = 0; tt < 3; ++tt) SN[1][tt] = mfma(kf[7], qf[3][tt], SN[1][tt]);
    }
    SB();
    if (__builtin_amdgcn_readfirstlane(flagv)) {
      float m[3];
      head_max(SC, m);
#pragma unroll
      for (int tt = 0; tt < 3; ++tt) {
        const float d = fmaxf(m[tt], 0.f);
        const float alpha = ex2(-d);
        const f32x4 d4 = {d, d, d, d};
        lsum[tt] *= alpha;
#pragma unroll
        for (int dt = 0; dt < 4; ++dt) O[dt][tt] *= alpha;
        nm[tt] -= d4;
        SC[0][tt] -= d4;
        SC[1][tt] -= d4;
        if constexpr (!LAST) { SN[0][tt] -= d4; SN[1][tt] -= d4; }
        Pown[tt] = p_put(tt, p_unit(SC[0][tt]), p_unit(SC[1][tt]));
        ps[tt] = mfma(ones, Pown[tt], zero4);
      }
      lds_barrier();
    }
    SB();
#pragma unroll
    for (int tt = 0; tt < 3; ++tt) Ppar[tt] = lds_ld128((pw ^ 1024u) + tt * 2048);
#pragma unroll
    for (int dt = 0; dt < 4; ++dt) vg[dt] = v_frag((va[dt] ^ 8192u) + vo);
#pragma unroll
    for (int tt = 0; tt < 3; ++tt) lsum[tt] += ps[tt][0];
    SB();
    pv3(0, vf[0], Pown);
    if constexpr (!LAST) LDMA(req_issue(IC(0), rq));
    SB();
    pv3(1, vf[1], Pown);
    if constexpr (!LAST) LDMA(req_issue(IC(1), rq));
    SB();
    pv3(2, vf[2], Pown);
    if constexpr (!LAST) LDMA(req_issue(IC(2), rq));
    SB();
    pv3(3, vf[3], Pown);
    if constexpr (!LAST) LDMA(req_issue(IC(3), rq));
    SB();
    pv3(0, vg[0], Ppar);
    pv3(1, vg[1], Ppar);
    if constexpr (LAST) {
      pv3(2, vg[2], Ppar);
      pv3(3, vg[3], Ppar);
    }
    SB();
  };
  auto wsrc = [&](int wi) { return wobase + (size_t)(wi >> 1) * 128 * 1024 + (wi & 1) * 256; };
  #define LAUNDER(v) asm volatile("" : "+v"(v))
  auto reqV = [&](int t) { LAUNDER(vxb); return Req{vbase + (size_t)t * 32768, 0u, 0x80u, 0u, 0x80u, vxb, false}; };
  auto reqK = [&](int t) { LAUNDER(vyb); return Req{kbase + (size_t)t * 32768, 0u, 0xC0u, 0x80u, 0x40u, vyb, false}; };
  auto reqW = [&](int wi) { LAUNDER(wvb); return Req{wsrc(wi), 3072u, 0x80u, 0u, 0x80u, wvb, true}; };

#pragma unroll 1
  for (int t2 = 0; t2 < 6; t2 += 2) {
    wait_barrier<8>();
    STAMP(3 + 2 * t2);
    if (t2 == 0) do_touch();
    xphase(IC(1), IC(3), std::false_type{}, SA, SB_, reqV(t2 + 1), t2);
    DSTAMP(25);
    wait_barrier<8>();
    STAMP(4 + 2 * t2);
    yphase(IC(2), IC(1), std::false_type{}, SA, SB_, reqK(t2 + 3), t2);
    DSTAMP(26);
    wait_barrier<8>();
    STAMP(5 + 2 * t2);
    xphase(IC(0), IC(2), std::false_type{}, SB_, SA, reqV(t2 + 2), t2 + 1);
    Req ry = t2 == 4 ? reqW(0) : reqK(t2 + 4);
    ry.shared = false;
    DSTAMP(27);
    wait_barrier<8>();
    STAMP(6 + 2 * t2);
    yphase(IC(3), IC(0), std::false_type{}, SB_, SA, ry, t2 + 1);
    DSTAMP(28);
  }
  wait_barrier<8>();
  STAMP(15);
  xphase(IC(1), IC(3), std::false_type{}, SA, SB_, reqV(7), 6);
  wait_barrier<8>();
  STAMP(16);
  yphase(IC(2), IC(1), std::false_type{}, SA, SB_, reqW(1), 6);
  wait_barrier<8>();
  STAMP(17);
  const unsigned epoch_v = __hip_atomic_load(epoch_ptr, __ATOMIC_RELAXED, __HIP_MEMORY_SCOPE_AGENT);
  SB();
  xphase(IC(0), IC(2), std::true_type{}, SB_, SA, reqW(2), 7);
  wait_barrier<8>();
  STAMP(18);
  yphase(IC(3), IC(3), std::true_type{}, SB_, SA, reqW(3), 7);

#pragma unroll
  for (int t = 0; t < 3; ++t)
    if (lane < 16) lds_stf(LS_OFF + ((hh * 4 + jq) * 3 + t) * 64 + lane * 4, lsum[t]);
  wait_barrier<0>();
  const unsigned tag = (unsigned)__builtin_amdgcn_readfirstlane(epoch_v) + 1u;
  SB();
  STAMPR(19);
  const unsigned ow = PB + hh * 12288 + (2 * (jq & 1) + (jq >> 1)) * 3072 + lane * 16;
  const unsigned ox = PB + hh * 12288 + (2 * (jq & 1) + ((jq >> 1) ^ 1)) * 3072 + lane * 16;
  f32x4 Om[2][3];
  float lt[3];
  {
#pragma unroll
    for (int t = 0; t < 3; ++t) lds_st128(ox + t * 1024, __builtin_bit_cast(u32x4, O[2][t]));
#pragma unroll
    for (int t = 0; t < 3; ++t) lds_st128(3 * SLOT + (w4k ^ 8192u) + lane * 16 + t * 1024, __builtin_bit_cast(u32x4, O[3][t]));
#pragma unroll
    for (int t = 0; t < 3; ++t) {
      float s = 0.f;
#pragma unroll
      for (int j = 0; j < 4; ++j) s += lds_ldf(LS_OFF + ((hh * 4 + j) * 3 + t) * 64 + i16 * 4);
      lt[t] = s;
    }
    lds_barrier();
#pragma unroll
    for (int t = 0; t < 3; ++t) Om[1][t] = O[1][t] + __builtin_bit_cast(f32x4, lds_ld128(3 * SLOT + w4k + lane * 16 + t * 1024));
#pragma unroll
    for (int t = 0; t < 3; ++t) Om[0][t] = O[0][t] + __builtin_bit_cast(f32x4, lds_ld128(ow + t * 1024));
    asm volatile("s_waitcnt lgkmcnt(0)" ::: "memory");
    SB();
    set_m0(w4k + 3 * SLOT);
    static_for<0, 4>([&](auto ic) { req_issue(ic, reqW(3)); });
    SB();
  }

  STAMP(34);
  const __amdgpu_buffer_rsrc_t ws_rs = __builtin_amdgcn_make_buffer_rsrc((void*)ws, 0, WS_END, 0x00020000);
  const unsigned reg1 = WS_IN * (unsigned)(b + 1), reg2 = reg1 + WS_IN2;
  const unsigned lbid = (unsigned)bid & 31u;

  float lam[3];
  u32x4 mine[3];
#pragma unroll
  for (int t = 0; t < 3; ++t) {
    const float inv = __builtin_amdgcn_rcpf(lt[t]);
    lam[t] = __builtin_amdgcn_logf(lt[t]) - nm[t][0];
#pragma unroll
    for (int dt = 0; dt < 2; ++dt) Om[dt][t] *= inv;
    mine[t] = u32x4{pk_bf16(Om[0][t][0], Om[0][t][1]), pk_bf16(Om[0][t][2], Om[0][t][3]), pk_bf16(Om[1][t][0], Om[1][t][1]),
                    pk_bf16(Om[1][t][2], Om[1][t][3])};
  }
  {
    const float l0 = lam[0], l1 = lam[1], l2 = lam[2];
    const unsigned lw = __builtin_bit_cast(unsigned, (lane & 32) ? l2 : ((lane & 16) ? l1 : l0));
    unsigned h1, h2;
    hash13(mine[0], mine[1], mine[2], lw, tag, h1, h2);
    const unsigned dst = reg1 + (lbid ^ 16u) * 32768u + (unsigned)(w * 4096 + lane * 16);
    __builtin_amdgcn_raw_buffer_store_b128(mine[0], ws_rs, dst, 0, 16);
    __builtin_amdgcn_raw_buffer_store_b128(mine[1], ws_rs, dst + 1024, 0, 16);
    __builtin_amdgcn_raw_buffer_store_b128(mine[2], ws_rs, dst + 2048, 0, 16);
    __builtin_amdgcn_raw_buffer_store_b128(u32x4{lw, h1, h2, tag}, ws_rs, dst + 3072, 0, 16);
    SB();
    if (lane == 0) __builtin_amdgcn_raw_buffer_store_b32(tag, ws_rs, WS_HINT1 + (unsigned)(bid ^ 16) * H1W + (unsigned)w * H1S, 0, 16);
  }
  STAMP(35);
  unsigned wa_[2];
  {
    const int q_ = (lane >> 2) & 3, p = lane & 3;
#pragma unroll
    for (int dt = 0; dt < 2; ++dt)
      wa_[dt] = hh * SLOT + (4 * g + q_) * 256 + (((4 * jq + 2 * dt + (p >> 1)) ^ (2 * ((4 * g + q_) & 7))) << 4) + 8 * (p & 1);
  }
  u32x4 wf[8][2];
#pragma unroll
  for (int st = 0; st < 4; ++st)
#pragma unroll
    for (int dt = 0; dt < 2; ++dt) wf[st][dt] = v_frag(wa_[dt] + st * 8192);
  SB();
  u32x4 th[4];
  STAMP(36);
  recv4<1024, 37, HOP1_AUX>(ws_rs, WS_HINT1 + (unsigned)bid * H1W + (unsigned)w * H1S, reg1 + lbid * 32768u + (unsigned)(w * 4096 + lane * 16), tag, th, lane, w);
  STAMPR(20);
  {
    const unsigned u = th[3][0];
    auto a = __builtin_amdgcn_permlane32_swap(u, u, false, false);
    auto c0 = __builtin_amdgcn_permlane16_swap(a[0], a[0], false, false);
    auto c1 = __builtin_amdgcn_permlane16_swap(a[1], a[1], false, false);
    const float lb[3] = {u2f(c0[0]), u2f(c0[1]), u2f(c1[0])};
#pragma unroll
    for (int t = 0; t < 3; ++t) {
      const float mx = fmaxf(lam[t], lb[t]);
      float wa = ex2(lam[t] - mx), wb = ex2(lb[t] - mx);
      const float inv = __builtin_amdgcn_rcpf(wa + wb);
      wa *= inv;
      wb *= inv;
      float m[8];
#pragma unroll
      for (int k = 0; k < 4; ++k) {
        m[2 * k] = wa * Om[k >> 1][t][2 * (k & 1)] + wb * bf_lo(th[t][k]);
        m[2 * k + 1] = wa * Om[k >> 1][t][2 * (k & 1) + 1] + wb * bf_hi(th[t][k]);
      }
      lds_st128(ow + t * 1024, u32x4{pk_bf16(m[0], m[1]), pk_bf16(m[2], m[3]), pk_bf16(m[4], m[5]), pk_bf16(m[6], m[7])});
    }
  }
  wait_barrier<0>();
  STAMP(21);

  const unsigned pr = PB + lane * 16;
  f32x4 A[2][3];
#pragma unroll
  for (int dt = 0; dt < 2; ++dt)
#pragma unroll
    for (int t = 0; t < 3; ++t) A[dt][t] = zero4;
  {
    u32x4 pf[8][3];
#pragma unroll
    for (int st = 0; st < 4; ++st)
#pragma unroll
      for (int t = 0; t < 3; ++t) pf[st][t] = lds_ld128(pr + st * 3072 + t * 1024);
#pragma unroll
    for (int st = 4; st < 8; ++st) {
#pragma unroll
      for (int dt = 0; dt < 2; ++dt) wf[st][dt] = v_frag(wa_[dt] + 2 * SLOT + (st - 4) * 8192);
#pragma unroll
      for (int t = 0; t < 3; ++t) pf[st][t] = lds_ld128(pr + st * 3072 + t * 1024);
    }
    SB();
#pragma unroll
    for (int st = 0; st < 8; ++st)
#pragma unroll
      for (int dt = 0; dt < 2; ++dt)
#pragma unroll
        for (int t = 0; t < 3; ++t) A[dt][t] = mfma(wf[st][dt], pf[st][t], A[dt][t]);
  }
  STAMP(22);

  {
    u32x4 f[3];
#pragma unroll
    for (int t = 0; t < 3; ++t)
      f[t] = u32x4{pk_bf16(A[0][t][0], A[0][t][1]), pk_bf16(A[0][t][2], A[0][t][3]), pk_bf16(A[1][t][0], A[1][t][1]),
                   pk_bf16(A[1][t][2], A[1][t][3])};
    unsigned h1, h2;
    hash13(f[0], f[1], f[2], 0u, tag, h1, h2);
    const int rho = 2 * w + (i16 >> 3);
    const int rbid = (rho >> 1) + 8 * ((rho & 1) + 2 * kh + 4 * b);
    const int pidx = kvh * 2 + hp;
    const unsigned dst = reg2 + (unsigned)((rbid & 31) * 32768 + pidx * 2048 + (8 * g + (i16 & 7)) * 16);
    __builtin_amdgcn_raw_buffer_store_b128(f[0], ws_rs, dst, 0, 16);
    __builtin_amdgcn_raw_buffer_store_b128(f[1], ws_rs, dst + 512, 0, 16);
    __builtin_amdgcn_raw_buffer_store_b128(f[2], ws_rs, dst + 1024, 0, 16);
    __builtin_amdgcn_raw_buffer_store_b128(u32x4{0u, h1, h2, tag}, ws_rs, dst + 1536, 0, 16);
    SB();
    if ((lane & 55) == 0) __builtin_amdgcn_raw_buffer_store_b32(tag, ws_rs, WS_HINT2 + (unsigned)rbid * H2W + (unsigned)pidx * H2S, 0, 16);
  }
  {
    STAMP(39);
    recv4<512, 40, HOP2_AUX>(ws_rs, WS_HINT2 + (unsigned)bid * H2W + (unsigned)(tid >> 5) * H2S, reg2 + lbid * 32768u + (unsigned)((tid >> 5) * 2048 + (tid & 31) * 16), tag, th, lane, w);
    STAMPR(23);
#pragma unroll
    for (int t = 0; t < 3; ++t) {
      f32x4 r;
#pragma unroll
      for (int k = 0; k < 2; ++k) {
        const float a0 = half_sum(bf_lo(th[t][k])), a1 = half_sum(bf_hi(th[t][k]));
        const float b0 = half_sum(bf_lo(th[t][2 + k])), b1 = half_sum(bf_hi(th[t][2 + k]));
        r[2 * k] = lane < 32 ? a0 : b0;
        r[2 * k + 1] = lane < 32 ? a1 : b1;
      }
      lds_st128(w * 3072 + t * 1024 + lane * 16, __builtin_bit_cast(u32x4, r));
    }
  }
  lds_barrier();
  if (tid == 256 && xk == 0 && hp == 0 && kh == 0) __hip_atomic_store(epoch_ptr, tag, __ATOMIC_RELAXED, __HIP_MEMORY_SCOPE_AGENT);
  if (tid < 192) {
    const int t = tid >> 6;
    f32x4 s = __builtin_bit_cast(f32x4, lds_ld128(t * 1024 + lane * 16));
#pragma unroll
    for (int v = 1; v < 8; ++v) s += __builtin_bit_cast(f32x4, lds_ld128(v * 3072 + t * 1024 + lane * 16));
    const int rho = xk * 2 + hp;
    const int nt = lane >> 5, l32 = lane & 31, gg = l32 >> 3, q = 8 * (rho & 1) + (l32 & 7);
    const int col = 256 * kh + 32 * (rho >> 1) + 16 * nt + 4 * gg;
    u32x2 o = {pk_bf16(s[0], s[1]), pk_bf16(s[2], s[3])};
    *(u32x2*)(out + ((size_t)b * SQ + 16 * t + q) * DM + col) = o;
  }
  STAMPR(24);
  STAMP_DUMP();
}

}

extern "C" void kernel_launch(void* const* d_in, const int* in_sizes, int n_in, void* d_out, int out_size, void* d_ws,
                              size_t ws_size, hipStream_t stream) {
  (void)in_sizes; (void)n_in; (void)out_size; (void)ws_size;
  (void)hipFuncSetAttribute((const void*)gqa_fused_kernel, hipFuncAttributeMaxDynamicSharedMemorySize, LDS_BYTES);
  gqa_fused_kernel<<<256, 512, LDS_BYTES, stream>>>((const uint16_t*)d_in[0], (const uint16_t*)d_in[1], (const uint16_t*)d_in[2],
                                                    (const uint16_t*)d_in[3], (const uint16_t*)d_in[4], (uint16_t*)d_out,
                                                    (unsigned char*)d_ws);
}
```
